```python
import torch

def _softmax(x, axis=-1):
    return torch.softmax(x, dim=axis)
def _gelu(x):
    return torch.nn.functional.gelu(x, approximate="tanh")
def _silu(x):
    return torch.nn.functional.silu(x)
def _sigmoid(x):
    return torch.sigmoid(x)
def _layernorm(x, axis=-1, eps=1e-5):
    m = x.mean(dim=axis, keepdim=True)
    v = x.var(dim=axis, keepdim=True, unbiased=False)
    return (x - m) / torch.sqrt(v + eps)
def _scatter_add(tgt, idx, src, axis=0):
    return tgt.clone().index_add_(axis, idx, src)
def _segment_sum(src, seg, n):
    out = torch.zeros((n,) + src.shape[1:], dtype=src.dtype)
    return out.index_add_(0, seg, src)
def _pad(x, pw):
    flat = tuple(v for pair in reversed(pw) for v in pair)
    return torch.nn.functional.pad(x, flat)

kernel_name = 'concat_heads__mha_block_cached__mha_block_cached__near_ridge'

def setup_inputs():

    _w = (lambda a: (lambda t: t.to(torch.bfloat16) if t.is_floating_point() else t)(torch.as_tensor(a)))
    def _A_setup():
        B, Sq, D = 8, 240, 640
        H, Sk, Dh = 32, 1024, 128
        return {
            "x": _w((lambda *s: torch.randn(*s, dtype=torch.bfloat16) * 0.8)(B, Sq, D)),
            "k_cache": _w((lambda *s: torch.randn(*s, dtype=torch.bfloat16) * 0.8)(B, H, Sk, Dh)),
            "v_cache": _w((lambda *s: torch.randn(*s, dtype=torch.bfloat16) * 0.05)(B, H, Sk, Dh)),
            "wq": _w((lambda *s: torch.randn(*s, dtype=torch.bfloat16) * 0.8)(D, H * Dh) * (D ** -0.5)),
            "wo": _w((lambda *s: torch.randn(*s, dtype=torch.bfloat16) * 0.05)(H * Dh, D) * ((H * Dh) ** -0.5)),
        }
    _A_in = _A_setup()

    def _B_setup():
        B, Sq, D = 8, 240, 640
        H, Sk, Dh = 32, 1024, 128
        return {
            "x": _w((lambda *s: torch.randn(*s, dtype=torch.bfloat16) * 0.8)(B, Sq, D)),
            "k_cache": _w((lambda *s: torch.randn(*s, dtype=torch.bfloat16) * 0.8)(B, H, Sk, Dh)),
            "v_cache": _w((lambda *s: torch.randn(*s, dtype=torch.bfloat16) * 0.05)(B, H, Sk, Dh)),
            "wq": _w((lambda *s: torch.randn(*s, dtype=torch.bfloat16) * 0.8)(D, H * Dh) * (D ** -0.5)),
            "wo": _w((lambda *s: torch.randn(*s, dtype=torch.bfloat16) * 0.05)(H * Dh, D) * ((H * Dh) ** -0.5)),
        }
    _B_in = _B_setup()

    return {
        "A_x": _A_in["x"],
        "A_k_cache": _A_in["k_cache"],
        "A_v_cache": _A_in["v_cache"],
        "A_wq": _A_in["wq"],
        "A_wo": _A_in["wo"],
        "B_x": _B_in["x"],
        "B_k_cache": _B_in["k_cache"],
        "B_v_cache": _B_in["v_cache"],
        "B_wq": _B_in["wq"],
        "B_wo": _B_in["wo"],
    }

def reference(A_x, A_k_cache, A_v_cache, A_wq, A_wo, B_x, B_k_cache, B_v_cache, B_wq, B_wo):
    def _A_ref(x, k_cache, v_cache, wq, wo):
        B, Sq, D = x.shape
        H, Dh = 32, 128
        q = (lambda x, *d: x.permute(*d))((x @ wq).reshape(B, Sq, H, Dh), 0, 2, 1, 3)
        scores = torch.einsum("bhqd,bhkd->bhqk", q, k_cache) * (Dh ** -0.5)
        p = _softmax(scores, axis=-1)
        o = torch.einsum("bhqk,bhkd->bhqd", p, v_cache)
        return (lambda x, *d: x.permute(*d))(o, 0, 2, 1, 3).reshape(B, Sq, H * Dh) @ wo

    def _B_ref(x, k_cache, v_cache, wq, wo):
        B, Sq, D = x.shape
        H, Dh = 32, 128
        q = (lambda x, *d: x.permute(*d))((x @ wq).reshape(B, Sq, H, Dh), 0, 2, 1, 3)
        scores = torch.einsum("bhqd,bhkd->bhqk", q, k_cache) * (Dh ** -0.5)
        p = _softmax(scores, axis=-1)
        o = torch.einsum("bhqk,bhkd->bhqd", p, v_cache)
        return (lambda x, *d: x.permute(*d))(o, 0, 2, 1, 3).reshape(B, Sq, H * Dh) @ wo

    a = _A_ref(A_x, A_k_cache, A_v_cache, A_wq, A_wo)
    b = _B_ref(B_x, B_k_cache, B_v_cache, B_wq, B_wo)
    return torch.concatenate([a, b], axis=-1)
```

```cpp
#include <hip/hip_runtime.h>
#include <hip/hip_bf16.h>
#include <stdint.h>

typedef __attribute__((ext_vector_type(8))) short bf16x8;
typedef __attribute__((ext_vector_type(4))) short s16x4;
typedef __attribute__((ext_vector_type(16))) float f32x16;
typedef __attribute__((ext_vector_type(4))) unsigned u32x4;
typedef __attribute__((ext_vector_type(2))) unsigned u32x2;
typedef float f32x2_t __attribute__((ext_vector_type(2)));
typedef __bf16 bf16x2_t __attribute__((ext_vector_type(2)));
typedef __attribute__((address_space(3))) const char* lds_cptr;
typedef short v4i16_t __attribute__((ext_vector_type(4)));

#define DI __device__ __forceinline__
#define SBAR() __builtin_amdgcn_sched_barrier(0)
#define PIN(x) asm volatile("" : "+v"(x))
#define MFMA32(a, b, c) __builtin_amdgcn_mfma_f32_32x32x16_bf16((a), (b), (c), 0, 0, 0)
#define RFL(x) ((unsigned)__builtin_amdgcn_readfirstlane((int)(x)))

constexpr int NBATCH = 8, SQ = 240, DMODEL = 640, NH = 32, SK = 1024, DH = 128;
constexpr int MROWS = NBATCH * SQ;
constexpr int HD = NH * DH;
constexpr float QSCALE = 0.08838834764831845f * 1.4426950408889634f;

DI int crow(int r, int hi) { return (r & 3) + 8 * (r >> 2) + 4 * hi; }
DI unsigned cvtpk(float lo, float hi) {
  f32x2_t v = {lo, hi};
  bf16x2_t b = __builtin_convertvector(v, bf16x2_t);
  return __builtin_bit_cast(unsigned, b);
}
DI void glds16(const void* g, unsigned lds_base) {
  unsigned sv;
  asm volatile("s_mov_b32 %0, m0\n\ts_mov_b32 m0, %2\n\ts_nop 0\n\tglobal_load_lds_dwordx4 %1, off\n\ts_mov_b32 m0, %0"
               : "=&s"(sv) : "v"(g), "s"(lds_base) : "memory");
}
DI s16x4 vtr(lds_cptr p) {
  return __builtin_bit_cast(s16x4, __builtin_amdgcn_ds_read_tr16_b64_v4i16((__attribute__((address_space(3))) v4i16_t*)p));
}
DI bf16x8 comb(s16x4 lo, s16x4 hi) { return (bf16x8){lo[0], lo[1], lo[2], lo[3], hi[0], hi[1], hi[2], hi[3]}; }
#define MX3(a, b, c) __builtin_fmaxf(__builtin_fmaxf((a), (b)), (c))
DI float rowmax32(const f32x16& p0, const f32x16& p1) {
  float a = MX3(p0[0], p0[1], p1[0]), b = MX3(p0[2], p0[3], p1[1]);
  a = MX3(a, p1[2], p1[3]);
#pragma unroll
  for (int r = 4; r < 16; r += 4) {
    a = MX3(a, p0[r], p0[r + 1]); b = MX3(b, p0[r + 2], p0[r + 3]);
    a = MX3(a, p1[r], p1[r + 1]); b = MX3(b, p1[r + 2], p1[r + 3]);
  }
  float m = __builtin_fmaxf(a, b);
  auto rr = __builtin_amdgcn_permlane32_swap(__float_as_uint(m), __float_as_uint(m), false, false);
  return __builtin_fmaxf(__uint_as_float(rr[0]), __uint_as_float(rr[1]));
}

constexpr int KRING = 0, VRING = 65536, WQOFF = 131072, LDS_BYTES = 163840;
constexpr float THR = 8.0f;

__global__ __launch_bounds__(512, 2) void attn_kernel(
    const uint16_t* __restrict__ xA, const uint16_t* __restrict__ kA, const uint16_t* __restrict__ vA, const uint16_t* __restrict__ wqA,
    const uint16_t* __restrict__ xB, const uint16_t* __restrict__ kB, const uint16_t* __restrict__ vB, const uint16_t* __restrict__ wqB,
    uint16_t* __restrict__ Ows) {
  __shared__ __attribute__((aligned(16))) char lds[LDS_BYTES];
  const int tid = threadIdx.x, lane = tid & 63, r32 = lane & 31, hi = lane >> 5;
  const int wid = __builtin_amdgcn_readfirstlane(tid >> 6);
  const int item = blockIdx.x;
  const int h = item & 31, b = (item >> 5) & 7, blk = item >> 8;
  const uint16_t* x = blk ? xB : xA;
  const uint16_t* wq = blk ? wqB : wqA;
  const uint16_t* Kb = (blk ? kB : kA) + (size_t)(b * NH + h) * SK * DH;
  const uint16_t* Vb = (blk ? vB : vA) + (size_t)(b * NH + h) * SK * DH;
  const unsigned lds0 = (unsigned)(uintptr_t)lds;

  const int row8 = (lane & 31) >> 2, c4 = lane & 3, sub = lane >> 5;
  const int vrd = ((lane & 3) << 3) | (((lane >> 2) & 3) << 6) | (((lane >> 4) & 1) << 5) | ((lane >> 5) << 8);

  bf16x8 qr[8];
  {
    const int kk = 8 * wid + row8;
    const int kx = (kk & ~12) | ((kk & 4) << 1) | ((kk & 8) >> 1);
    const uint16_t* wsrc = wq + (size_t)kx * HD + h * DH + 32 * sub + 8 * c4;
#define DMA_W(j) do { glds16(wsrc + (size_t)(j) * 64 * HD,      RFL(lds0 + WQOFF + ((j) & 1) * 16384 + (2 * wid) * 1024)); \
                      glds16(wsrc + (size_t)(j) * 64 * HD + 64, RFL(lds0 + WQOFF + ((j) & 1) * 16384 + (2 * wid + 1) * 1024)); } while (0)
    int qrow = 32 * wid + r32; if (qrow > SQ - 1) qrow = SQ - 1;
    const uint16_t* xrow = x + ((size_t)b * SQ + qrow) * DMODEL + 8 * hi;
    f32x16 qa[4];
#pragma unroll
    for (int i = 0; i < 4; ++i) qa[i] = f32x16{};
    DMA_W(0);
    bf16x8 xf[4];
#pragma unroll
    for (int ks = 0; ks < 4; ++ks) xf[ks] = *(const bf16x8*)(xrow + 16 * ks);
    for (int j = 0; j < 10; ++j) {
      asm volatile("s_waitcnt vmcnt(0)\n\ts_barrier" ::: "memory");
      if (j + 1 < 10) DMA_W(j + 1);
      bf16x8 xn[4];
      const int jn = (j + 1 < 10) ? j + 1 : j;
#pragma unroll
      for (int ks = 0; ks < 4; ++ks) xn[ks] = *(const bf16x8*)(xrow + 64 * jn + 16 * ks);
      const lds_cptr wp = (lds_cptr)lds + WQOFF + (j & 1) * 16384 + vrd;
#pragma unroll
      for (int ks = 0; ks < 4; ++ks) {
#pragma unroll
        for (int db = 0; db < 4; ++db) {
          s16x4 lo = vtr(wp + ks * 4096 + db * 512), hh = vtr(wp + ks * 4096 + 2048 + db * 512);
          qa[db] = MFMA32(comb(lo, hh), xf[ks], qa[db]);
        }
      }
#pragma unroll
      for (int ks = 0; ks < 4; ++ks) xf[ks] = xn[ks];
    }
#undef DMA_W
#pragma unroll
    for (int t = 0; t < 4; ++t) {
#pragma unroll
      for (int e = 0; e < 2; ++e) {
        const int B0 = 8 * e;
        unsigned a0 = cvtpk(qa[t][B0 + 0] * QSCALE, qa[t][B0 + 1] * QSCALE), a1 = cvtpk(qa[t][B0 + 2] * QSCALE, qa[t][B0 + 3] * QSCALE);
        unsigned b0 = cvtpk(qa[t][B0 + 4] * QSCALE, qa[t][B0 + 5] * QSCALE), b1 = cvtpk(qa[t][B0 + 6] * QSCALE, qa[t][B0 + 7] * QSCALE);
        auto r0 = __builtin_amdgcn_permlane32_swap(a0, b0, false, false);
        auto r1 = __builtin_amdgcn_permlane32_swap(a1, b1, false, false);
        u32x4 w = {r0[0], r1[0], r0[1], r1[1]};
        qr[2 * t + e] = __builtin_bit_cast(bf16x8, w);
      }
    }
  }

  const uint16_t* ksrc[2];
  const uint16_t* vsrc[2];
#pragma unroll
  for (int m = 0; m < 2; ++m) {
    const int krow = 8 * wid + 4 * m + (lane >> 4);
    ksrc[m] = Kb + (size_t)krow * DH + (((lane & 15) ^ (krow & 15)) * 8);
    vsrc[m] = Vb + (size_t)(8 * wid + row8) * DH + 64 * m + 32 * sub + 8 * c4;
  }
  const unsigned kdst = lds0 + KRING + (2 * wid) * 1024, vdst = lds0 + VRING + (2 * wid) * 1024;
#define DMA_K(t) do { glds16(ksrc[0] + (size_t)(t) * 64 * DH, RFL(kdst + ((t) & 3) * 16384)); glds16(ksrc[1] + (size_t)(t) * 64 * DH, RFL(kdst + ((t) & 3) * 16384 + 1024)); } while (0)
#define DMA_V(t) do { glds16(vsrc[0] + (size_t)(t) * 64 * DH, RFL(vdst + ((t) & 3) * 16384)); glds16(vsrc[1] + (size_t)(t) * 64 * DH, RFL(vdst + ((t) & 3) * 16384 + 1024)); } while (0)
  int kaddr[8];
#pragma unroll
  for (int d0 = 0; d0 < 8; ++d0) kaddr[d0] = r32 * 256 + (((2 * d0 + hi) ^ (r32 & 15)) << 4);

  DMA_K(0); DMA_K(1); DMA_V(0); DMA_K(2); DMA_V(1);

  f32x16 o[4];
#pragma unroll
  for (int i = 0; i < 4; ++i) o[i] = f32x16{};
  float mhat = 0.f, l_reg = 0.f, fres = 1.f;
  bool resc = false;
  f32x16 pA0, pA1, pB0, pB1;
  bf16x8 kf[3];
  s16x4 vlo[3], vhi[3];
  u32x4 pw[4];

#define KLD(i) (*(const __attribute__((address_space(3))) bf16x8*)(kp_ + kaddr[(i) >> 1] + ((i) & 1) * 8192))
#define VRD(i) do { vlo[(i) % 3] = vtr(vp_ + ((i) >> 2) * 4096 + ((i) & 3) * 512); vhi[(i) % 3] = vtr(vp_ + ((i) >> 2) * 4096 + 2048 + ((i) & 3) * 512); } while (0)
#define PACKP(P0, P1, i) do { if ((i) < 8) pw[(i) >> 2][(i) & 3] = cvtpk(P0[2 * (i)], P0[2 * (i) + 1]); \
                              else pw[2 + (((i) - 8) >> 2)][((i) - 8) & 3] = cvtpk(P1[2 * ((i) - 8)], P1[2 * ((i) - 8) + 1]); } while (0)
#define QKM(C0, C1, i) do { if ((i) & 1) { C1 = MFMA32(kf[(i) % 3], qr[(i) >> 1], ((i) < 2) ? f32x16{} : C1); } \
                            else { C0 = MFMA32(kf[(i) % 3], qr[(i) >> 1], ((i) < 2) ? f32x16{} : C0); } } while (0)
#define GAPA(C0, C1, P0, P1, i) do { if ((i) + 2 < 16) kf[((i) + 2) % 3] = KLD((i) + 2); SBAR(); \
    QKM(C0, C1, i); s0 += P0[i]; s1 += P1[i]; PACKP(P0, P1, i); PIN(pw[(i) >> 2]); PIN(s0); PIN(s1); SBAR(); } while (0)
#define EXP1(v) __builtin_amdgcn_exp2f((v) - mh)
#define GAPB(C0, C1, i) do { if ((i) + 2 < 16) VRD((i) + 2); SBAR(); \
    o[(i) & 3] = MFMA32(comb(vlo[(i) % 3], vhi[(i) % 3]), __builtin_bit_cast(bf16x8, pw[(i) >> 2]), o[(i) & 3]); \
    if ((i) < 8) { C0[2 * (i)] = EXP1(C0[2 * (i)]); C0[2 * (i) + 1] = EXP1(C0[2 * (i) + 1]); PIN(C0); } \
    else { C1[2 * ((i) - 8)] = EXP1(C1[2 * ((i) - 8)]); C1[2 * ((i) - 8) + 1] = EXP1(C1[2 * ((i) - 8) + 1]); PIN(C1); } SBAR(); } while (0)
#define REP16(M, ...) M(__VA_ARGS__, 0); M(__VA_ARGS__, 1); M(__VA_ARGS__, 2); M(__VA_ARGS__, 3); M(__VA_ARGS__, 4); M(__VA_ARGS__, 5); M(__VA_ARGS__, 6); M(__VA_ARGS__, 7); \
                      M(__VA_ARGS__, 8); M(__VA_ARGS__, 9); M(__VA_ARGS__, 10); M(__VA_ARGS__, 11); M(__VA_ARGS__, 12); M(__VA_ARGS__, 13); M(__VA_ARGS__, 14); M(__VA_ARGS__, 15)

  asm volatile("s_waitcnt vmcnt(8)\n\ts_barrier" ::: "memory");
  DMA_K(3); DMA_V(2);
  {
    const lds_cptr kp_ = (lds_cptr)lds + KRING;
#pragma unroll
    for (int i = 0; i < 16; ++i) { kf[i % 3] = KLD(i); QKM(pA0, pA1, i); }
    const float rm = rowmax32(pA0, pA1);
    mhat = rm;
    const float mh = mhat;
#pragma unroll
    for (int r = 0; r < 16; ++r) { pA0[r] = EXP1(pA0[r]); pA1[r] = EXP1(pA1[r]); }
  }

#define STEP(C0, C1, P0, P1, t, GK, GV) do { \
    const lds_cptr kp_ = (lds_cptr)lds + KRING + ((t) & 3) * 16384; \
    const lds_cptr vp_ = (lds_cptr)lds + VRING + (((t) - 1) & 3) * 16384 + vrd; \
    kf[0] = KLD(0); kf[1] = KLD(1); \
    if (GK) DMA_K((t) + 3); if (GV) DMA_V((t) + 2); \
    float s0 = 0.f, s1 = 0.f; SBAR(); \
    REP16(GAPA, C0, C1, P0, P1); \
    VRD(0); VRD(1); \
    l_reg += s0 + s1; \
    { const float rm = rowmax32(C0, C1) - mhat; resc = false; \
      if (__builtin_expect(__any(rm > THR), 0)) { const float dl = __builtin_fmaxf(rm, 0.f); mhat += dl; \
          fres = __builtin_amdgcn_exp2f(-dl); l_reg *= fres; resc = true; } } \
    const float mh = mhat; SBAR(); \
    REP16(GAPB, C0, C1); \
    if (resc) { _Pragma("unroll") for (int d_ = 0; d_ < 4; ++d_) _Pragma("unroll") for (int r = 0; r < 16; ++r) o[d_][r] *= fres; } \
  } while (0)
#define WAITBAR(N) asm volatile("s_waitcnt vmcnt(" #N ")\n\ts_barrier" ::: "memory")

  WAITBAR(8);
  for (int t = 1; t < 13; t += 2) {
    STEP(pB0, pB1, pA0, pA1, t, true, true);     WAITBAR(8);
    STEP(pA0, pA1, pB0, pB1, t + 1, true, true); WAITBAR(8);
  }
  STEP(pB0, pB1, pA0, pA1, 13, false, true);  WAITBAR(6);
  STEP(pA0, pA1, pB0, pB1, 14, false, false); WAITBAR(2);
  STEP(pB0, pB1, pA0, pA1, 15, false, false); WAITBAR(0);
  {
    float s0 = 0.f, s1 = 0.f;
#pragma unroll
    for (int i = 0; i < 16; ++i) { s0 += pB0[i]; s1 += pB1[i]; PACKP(pB0, pB1, i); }
    l_reg += s0 + s1;
    const lds_cptr vp_ = (lds_cptr)lds + VRING + (15 & 3) * 16384 + vrd;
#pragma unroll
    for (int i = 0; i < 16; ++i) {
      VRD(i);
      o[i & 3] = MFMA32(comb(vlo[i % 3], vhi[i % 3]), __builtin_bit_cast(bf16x8, pw[i >> 2]), o[i & 3]);
    }
  }
  {
    auto rr = __builtin_amdgcn_permlane32_swap(__float_as_uint(l_reg), __float_as_uint(l_reg), false, false);
    l_reg = __uint_as_float(rr[0]) + __uint_as_float(rr[1]);
  }
  const float rinv = 1.0f / l_reg;
  const int q = 32 * wid + r32;
  if (q < SQ) {
    uint16_t* dst = Ows + ((size_t)blk * MROWS + (size_t)b * SQ + q) * HD + h * DH + 4 * hi;
#pragma unroll
    for (int db = 0; db < 4; ++db) {
#pragma unroll
      for (int g = 0; g < 4; ++g) {
        u32x2 w;
        w[0] = cvtpk(o[db][4 * g + 0] * rinv, o[db][4 * g + 1] * rinv);
        w[1] = cvtpk(o[db][4 * g + 2] * rinv, o[db][4 * g + 3] * rinv);
        *(u32x2*)(dst + 32 * db + 8 * g) = w;
      }
    }
  }
#undef DMA_K
#undef DMA_V
#undef KLD
#undef VRD
#undef PACKP
#undef QKM
#undef GAPA
#undef EXP1
#undef GAPB
#undef STEP
#undef WAITBAR
}

constexpr int G_BM = 128, G_BN = 64, G_BK = 64;
constexpr int G_AB = G_BM * G_BK * 2, G_BB = G_BK * G_BN * 2, G_STAGE = G_AB + G_BB;

__global__ __launch_bounds__(256) void oproj_kernel(const uint16_t* __restrict__ Ows, const uint16_t* __restrict__ woA,
                                                   const uint16_t* __restrict__ woB, uint16_t* __restrict__ out) {
  __shared__ __attribute__((aligned(16))) char lds[2 * G_STAGE];
  const int tid = threadIdx.x, lane = tid & 63, r32 = lane & 31, hi = lane >> 5;
  const int wid = __builtin_amdgcn_readfirstlane(tid >> 6);
  const int nt = blockIdx.x, mt = blockIdx.y, blk = blockIdx.z;
  const uint16_t* A = Ows + ((size_t)blk * MROWS + (size_t)mt * G_BM) * HD;
  const uint16_t* Bw = (blk ? woB : woA) + nt * G_BN;
  const unsigned lds0 = (unsigned)(uintptr_t)lds;
  const int wm = wid >> 1, wn = wid & 1;
  const uint16_t* asrc[4];
#pragma unroll
  for (int i = 0; i < 4; ++i) {
    const int row = 8 * (4 * wid + i) + (lane >> 3);
    asrc[i] = A + (size_t)row * HD + (((lane & 7) ^ ((row >> 1) & 7)) * 8);
  }
  const int row8 = (lane & 31) >> 2, c4 = lane & 3, sub = lane >> 5;
  const uint16_t* bsrc[2];
#pragma unroll
  for (int i = 0; i < 2; ++i) {
    const int kk = 8 * (2 * wid + i) + row8;
    const int kx = (kk & ~12) | ((kk & 4) << 1) | ((kk & 8) >> 1);
    bsrc[i] = Bw + (size_t)kx * DMODEL + 32 * sub + 8 * c4;
  }
#define G_STG(kt, buf) do { _Pragma("unroll") for (int i = 0; i < 4; ++i) glds16(asrc[i] + (size_t)(kt) * G_BK, RFL(lds0 + (buf) * G_STAGE + (4 * wid + i) * 1024)); \
    _Pragma("unroll") for (int i = 0; i < 2; ++i) glds16(bsrc[i] + (size_t)(kt) * G_BK * DMODEL, RFL(lds0 + (buf) * G_STAGE + G_AB + (2 * wid + i) * 1024)); } while (0)
  const int vrd = ((lane & 3) << 3) | (((lane >> 2) & 3) << 6) | (((lane >> 4) & 1) << 5) | ((lane >> 5) << 8);
  int aaddr[2][4];
#pragma unroll
  for (int mb = 0; mb < 2; ++mb)
#pragma unroll
    for (int ks = 0; ks < 4; ++ks) {
      const int row = 64 * wm + 32 * mb + r32;
      aaddr[mb][ks] = row * 128 + (((2 * ks + hi) ^ ((row >> 1) & 7)) << 4);
    }
  f32x16 acc[2];
  acc[0] = f32x16{}; acc[1] = f32x16{};
  constexpr int NKT = HD / G_BK;
  G_STG(0, 0);
  for (int kt = 0; kt < NKT; ++kt) {
    const int buf = kt & 1;
    asm volatile("s_waitcnt vmcnt(0)\n\ts_barrier" ::: "memory");
    if (kt + 1 < NKT) G_STG(kt + 1, buf ^ 1);
    const lds_cptr ap = (lds_cptr)lds + buf * G_STAGE;
    const lds_cptr bp = (lds_cptr)lds + buf * G_STAGE + G_AB + vrd + wn * 512;
#pragma unroll
    for (int ks = 0; ks < 4; ++ks) {
      s16x4 lo = vtr(bp + ks * 2048), hh = vtr(bp + ks * 2048 + 1024);
      bf16x8 bfr = comb(lo, hh);
#pragma unroll
      for (int mb = 0; mb < 2; ++mb) {
        bf16x8 afr = *(const __attribute__((address_space(3))) bf16x8*)(ap + aaddr[mb][ks]);
        acc[mb] = MFMA32(bfr, afr, acc[mb]);
      }
    }
  }
#undef G_STG
#pragma unroll
  for (int mb = 0; mb < 2; ++mb) {
    const int m = mt * G_BM + 64 * wm + 32 * mb + r32;
    uint16_t* dst = out + (size_t)m * (2 * DMODEL) + blk * DMODEL + nt * G_BN + 32 * wn + 4 * hi;
#pragma unroll
    for (int g = 0; g < 4; ++g) {
      u32x2 w;
      w[0] = cvtpk(acc[mb][4 * g + 0], acc[mb][4 * g + 1]);
      w[1] = cvtpk(acc[mb][4 * g + 2], acc[mb][4 * g + 3]);
      *(u32x2*)(dst + 8 * g) = w;
    }
  }
}

extern "C" void kernel_launch(void* const* d_in, const int* in_sizes, int n_in, void* d_out, int out_size, void* d_ws, size_t ws_size,
                              hipStream_t stream) {
  const uint16_t* xA = (const uint16_t*)d_in[0];
  const uint16_t* kA = (const uint16_t*)d_in[1];
  const uint16_t* vA = (const uint16_t*)d_in[2];
  const uint16_t* wqA = (const uint16_t*)d_in[3];
  const uint16_t* woA = (const uint16_t*)d_in[4];
  const uint16_t* xB = (const uint16_t*)d_in[5];
  const uint16_t* kB = (const uint16_t*)d_in[6];
  const uint16_t* vB = (const uint16_t*)d_in[7];
  const uint16_t* wqB = (const uint16_t*)d_in[8];
  const uint16_t* woB = (const uint16_t*)d_in[9];
  uint16_t* Ows = (uint16_t*)d_ws;
  attn_kernel<<<dim3(512), dim3(512), 0, stream>>>(xA, kA, vA, wqA, xB, kB, vB, wqB, Ows);
  oproj_kernel<<<dim3(DMODEL / G_BN, MROWS / G_BM, 2), dim3(256), 0, stream>>>(Ows, woA, woB, (uint16_t*)d_out);
}
```

```cpp
#include <hip/hip_runtime.h>
#include <hip/hip_bf16.h>
#include <stdint.h>

typedef __attribute__((ext_vector_type(8))) short bf16x8;
typedef __attribute__((ext_vector_type(4))) short s16x4;
typedef __attribute__((ext_vector_type(16))) float f32x16;
typedef __attribute__((ext_vector_type(4))) unsigned u32x4;
typedef __attribute__((ext_vector_type(2))) unsigned u32x2;
typedef float f32x2_t __attribute__((ext_vector_type(2)));
typedef __bf16 bf16x2_t __attribute__((ext_vector_type(2)));
typedef __attribute__((address_space(3))) const char* lds_cptr;
typedef short v4i16_t __attribute__((ext_vector_type(4)));

#define DI __device__ __forceinline__
#define SBAR() __builtin_amdgcn_sched_barrier(0)
#define PIN(x) asm volatile("" : "+v"(x))
#define MFMA32(a, b, c) __builtin_amdgcn_mfma_f32_32x32x16_bf16((a), (b), (c), 0, 0, 0)
#define RFL(x) ((unsigned)__builtin_amdgcn_readfirstlane((int)(x)))

constexpr int NBATCH = 8, SQ = 240, DMODEL = 640, NH = 32, SK = 1024, DH = 128;
constexpr int MROWS = NBATCH * SQ;
constexpr int HD = NH * DH;
constexpr float QSCALE = 0.08838834764831845f * 1.4426950408889634f;

#define STAMP(i) do {} while (0)
DI int crow(int r, int hi) { return (r & 3) + 8 * (r >> 2) + 4 * hi; }
DI unsigned cvtpk(float lo, float hi) {
  f32x2_t v = {lo, hi};
  bf16x2_t b = __builtin_convertvector(v, bf16x2_t);
  return __builtin_bit_cast(unsigned, b);
}
DI void glds16(const void* g, unsigned lds_base) {
  unsigned sv;
  asm volatile("s_mov_b32 %0, m0\n\ts_mov_b32 m0, %2\n\ts_nop 0\n\tglobal_load_lds_dwordx4 %1, off\n\ts_mov_b32 m0, %0"
               : "=&s"(sv) : "v"(g), "s"(lds_base) : "memory");
}
DI s16x4 vtr(lds_cptr p) {
  return __builtin_bit_cast(s16x4, __builtin_amdgcn_ds_read_tr16_b64_v4i16((__attribute__((address_space(3))) v4i16_t*)p));
}
DI bf16x8 comb(s16x4 lo, s16x4 hi) { return (bf16x8){lo[0], lo[1], lo[2], lo[3], hi[0], hi[1], hi[2], hi[3]}; }
#define MX3(a, b, c) __builtin_fmaxf(__builtin_fmaxf((a), (b)), (c))
DI float rowmax32(const f32x16& p0, const f32x16& p1) {
  float a = MX3(p0[0], p0[1], p1[0]), b = MX3(p0[2], p0[3], p1[1]);
  a = MX3(a, p1[2], p1[3]);
#pragma unroll
  for (int r = 4; r < 16; r += 4) {
    a = MX3(a, p0[r], p0[r + 1]); b = MX3(b, p0[r + 2], p0[r + 3]);
    a = MX3(a, p1[r], p1[r + 1]); b = MX3(b, p1[r + 2], p1[r + 3]);
  }
  float m = __builtin_fmaxf(a, b);
  auto rr = __builtin_amdgcn_permlane32_swap(__float_as_uint(m), __float_as_uint(m), false, false);
  return __builtin_fmaxf(__uint_as_float(rr[0]), __uint_as_float(rr[1]));
}

constexpr int NSLOT = 10, SLOTB = 16384, LDS_BYTES = NSLOT * SLOTB;
constexpr float THR = 8.0f;

DI void glds16s(unsigned voff, const void* sbase, unsigned lds_base) {
  unsigned sv;
  asm volatile("s_mov_b32 %0, m0\n\ts_mov_b32 m0, %3\n\ts_nop 0\n\tglobal_load_lds_dwordx4 %1, %2\n\ts_mov_b32 m0, %0"
               : "=&s"(sv) : "v"(voff), "s"(sbase), "s"(lds_base) : "memory");
}
DI const void* uni(const void* p) {
  const unsigned long long v = (unsigned long long)p;
  const unsigned lo = RFL((unsigned)v), hi = RFL((unsigned)(v >> 32));
  return (const void*)(((unsigned long long)hi << 32) | lo);
}

struct LaneC {
  unsigned woff, koff0, koff1, voff0, voff1, xoff, dst0;
  int kaddr[8];
  int vrd;
};

template <int IT>
DI void attn_item(const char* lds, const LaneC& c, const int wid, const int hi,
                  const char* x, const char* Kp, const char* Vp, const char* wq_cur, const char* wq_next, const char* x_next,
                  bf16x8 (&xb0)[4], bf16x8 (&xb1)[4], bf16x8 (&xb2)[4], bf16x8 (&xb3)[4],
                  uint16_t* __restrict__ Odst, const bool qvalid, unsigned long long* stamp_buf) {
  constexpr bool has_next = (IT == 0);
#define SLOT(it_, n_) ((unsigned)(((2 * (it_) + (n_)) % NSLOT) * SLOTB))
#define DMA_W(W_, j, sl) do { glds16s(c.woff, (W_) + (size_t)(j) * 64 * HD * 2, RFL(c.dst0 + (sl))); glds16s(c.woff + 128, (W_) + (size_t)(j) * 64 * HD * 2, RFL(c.dst0 + (sl) + 1024)); } while (0)
#define DMA_K(K_, t, sl) do { glds16s(c.koff0, (K_) + (size_t)(t) * 64 * DH * 2, RFL(c.dst0 + (sl))); glds16s(c.koff1, (K_) + (size_t)(t) * 64 * DH * 2, RFL(c.dst0 + (sl) + 1024)); } while (0)
#define DMA_V(V_, t, sl) do { glds16s(c.voff0, (V_) + (size_t)(t) * 64 * DH * 2, RFL(c.dst0 + (sl))); glds16s(c.voff1, (V_) + (size_t)(t) * 64 * DH * 2, RFL(c.dst0 + (sl) + 1024)); } while (0)
#define XLD(dst, X_, off) asm volatile("global_load_dwordx4 %0, %1, %2 offset:%c3" : "=v"(dst) : "v"(c.xoff), "s"(X_), "i"(off) : "memory")
#define XLD4(buf, X_, j) do { XLD(buf[0], X_, 128 * (j)); XLD(buf[1], X_, 128 * (j) + 32); XLD(buf[2], X_, 128 * (j) + 64); XLD(buf[3], X_, 128 * (j) + 96); } while (0)
#define WAITX(N, buf) asm volatile("s_waitcnt vmcnt(" #N ")" : "+v"(buf[0]), "+v"(buf[1]), "+v"(buf[2]), "+v"(buf[3]) :: "memory")
#define BARRIER() asm volatile("s_barrier" ::: "memory")
#define WAITBAR(N) asm volatile("s_waitcnt vmcnt(" #N ")\n\ts_barrier" ::: "memory")
  STAMP(8 * IT + 0);
  bf16x8 qr[8];
  {
    f32x16 qa[4];
#pragma unroll
    for (int i = 0; i < 4; ++i) qa[i] = f32x16{};
#define QSTEP(j, XC, XN, NW, ISSUE) do { \
      WAITX(NW, XC); BARRIER(); \
      if ((j) + 3 < 10) XLD4(XN, x, (j) + 3); \
      ISSUE; \
      const lds_cptr wp = (lds_cptr)lds + SLOT(IT, j) + c.vrd; \
      _Pragma("unroll") for (int ks = 0; ks < 4; ++ks) { \
        _Pragma("unroll") for (int db = 0; db < 4; ++db) { \
          s16x4 lo = vtr(wp + ks * 4096 + db * 512), hh = vtr(wp + ks * 4096 + 2048 + db * 512); \
          qa[db] = MFMA32(comb(lo, hh), XC[ks], qa[db]); } } \
    } while (0)
    QSTEP(0, xb0, xb3, 8,  if (IT) DMA_W(wq_cur, 9, SLOT(1, 9)));
    if constexpr (IT == 0) {
      QSTEP(1, xb1, xb0, 8,  DMA_K(Kp, 0, SLOT(IT, 10)));
      QSTEP(2, xb2, xb1, 10, DMA_K(Kp, 1, SLOT(IT, 11)));
      QSTEP(3, xb3, xb2, 12, DMA_V(Vp, 0, SLOT(IT, 12)));
    } else {
      QSTEP(1, xb1, xb0, 14, DMA_K(Kp, 0, SLOT(IT, 10)));
      QSTEP(2, xb2, xb1, 14, DMA_K(Kp, 1, SLOT(IT, 11)));
      QSTEP(3, xb3, xb2, 14, DMA_V(Vp, 0, SLOT(IT, 12)));
    }
    QSTEP(4, xb0, xb3, 14, DMA_K(Kp, 2, SLOT(IT, 13)));
    QSTEP(5, xb1, xb0, 14, DMA_V(Vp, 1, SLOT(IT, 14)));
    QSTEP(6, xb2, xb1, 14, DMA_K(Kp, 3, SLOT(IT, 15)));
    QSTEP(7, xb3, xb2, 14, DMA_V(Vp, 2, SLOT(IT, 16)));
    QSTEP(8, xb0, xb3, 10, DMA_K(Kp, 4, SLOT(IT, 17)));
    QSTEP(9, xb1, xb0, 6,  DMA_V(Vp, 3, SLOT(IT, 18)));
#undef QSTEP
#pragma unroll
    for (int t = 0; t < 4; ++t) {
#pragma unroll
      for (int e = 0; e < 2; ++e) {
        const int B0 = 8 * e;
        unsigned a0 = cvtpk(qa[t][B0 + 0] * QSCALE, qa[t][B0 + 1] * QSCALE), a1 = cvtpk(qa[t][B0 + 2] * QSCALE, qa[t][B0 + 3] * QSCALE);
        unsigned b0 = cvtpk(qa[t][B0 + 4] * QSCALE, qa[t][B0 + 5] * QSCALE), b1 = cvtpk(qa[t][B0 + 6] * QSCALE, qa[t][B0 + 7] * QSCALE);
        auto r0 = __builtin_amdgcn_permlane32_swap(a0, b0, false, false);
        auto r1 = __builtin_amdgcn_permlane32_swap(a1, b1, false, false);
        u32x4 w = {r0[0], r1[0], r0[1], r1[1]};
        qr[2 * t + e] = __builtin_bit_cast(bf16x8, w);
      }
    }
  }
  STAMP(8 * IT + 1);

  f32x16 o[4];
#pragma unroll
  for (int i = 0; i < 4; ++i) o[i] = f32x16{};
  float mhat = 0.f, l_reg = 0.f, fres = 1.f;
  bool resc = false;
  f32x16 pA0, pA1, pB0, pB1;
  bf16x8 kf[3];
  s16x4 vlo[3], vhi[3];
  u32x4 pw[4];

#define KLD(i) (*(const __attribute__((address_space(3))) bf16x8*)(kp_ + c.kaddr[(i) >> 1] + ((i) & 1) * 8192))
#define VRD(i) do { vlo[(i) % 3] = vtr(vp_ + ((i) >> 2) * 4096 + ((i) & 3) * 512); vhi[(i) % 3] = vtr(vp_ + ((i) >> 2) * 4096 + 2048 + ((i) & 3) * 512); } while (0)
#define PACKP(P0, P1, i) do { if ((i) < 8) pw[(i) >> 2][(i) & 3] = cvtpk(P0[2 * (i)], P0[2 * (i) + 1]); \
                              else pw[2 + (((i) - 8) >> 2)][((i) - 8) & 3] = cvtpk(P1[2 * ((i) - 8)], P1[2 * ((i) - 8) + 1]); } while (0)
#define QKM(C0, C1, i) do { if ((i) & 1) { C1 = MFMA32(kf[(i) % 3], qr[(i) >> 1], ((i) < 2) ? f32x16{} : C1); } \
                            else { C0 = MFMA32(kf[(i) % 3], qr[(i) >> 1], ((i) < 2) ? f32x16{} : C0); } } while (0)
#define GAPA(C0, C1, P0, P1, i) do { if ((i) + 2 < 16) kf[((i) + 2) % 3] = KLD((i) + 2); SBAR(); \
    QKM(C0, C1, i); s0 += P0[i]; s1 += P1[i]; PACKP(P0, P1, i); PIN(pw[(i) >> 2]); PIN(s0); PIN(s1); SBAR(); } while (0)
#define EXP1(v) __builtin_amdgcn_exp2f((v) - mh)
#define GAPB(C0, C1, i) do { if ((i) + 2 < 16) VRD((i) + 2); SBAR(); \
    o[(i) & 3] = MFMA32(comb(vlo[(i) % 3], vhi[(i) % 3]), __builtin_bit_cast(bf16x8, pw[(i) >> 2]), o[(i) & 3]); \
    if ((i) < 8) { C0[2 * (i)] = EXP1(C0[2 * (i)]); C0[2 * (i) + 1] = EXP1(C0[2 * (i) + 1]); PIN(C0); } \
    else { C1[2 * ((i) - 8)] = EXP1(C1[2 * ((i) - 8)]); C1[2 * ((i) - 8) + 1] = EXP1(C1[2 * ((i) - 8) + 1]); PIN(C1); } SBAR(); } while (0)
#define REP16(M, ...) M(__VA_ARGS__, 0); M(__VA_ARGS__, 1); M(__VA_ARGS__, 2); M(__VA_ARGS__, 3); M(__VA_ARGS__, 4); M(__VA_ARGS__, 5); M(__VA_ARGS__, 6); M(__VA_ARGS__, 7); \
                      M(__VA_ARGS__, 8); M(__VA_ARGS__, 9); M(__VA_ARGS__, 10); M(__VA_ARGS__, 11); M(__VA_ARGS__, 12); M(__VA_ARGS__, 13); M(__VA_ARGS__, 14); M(__VA_ARGS__, 15)

  WAITBAR(16);
  DMA_K(Kp, 5, SLOT(IT, 19));
  STAMP(8 * IT + 2);
  {
    const lds_cptr kp_ = (lds_cptr)lds + SLOT(IT, 10);
#pragma unroll
    for (int i = 0; i < 16; ++i) { kf[i % 3] = KLD(i); QKM(pA0, pA1, i); }
    const float rm = rowmax32(pA0, pA1);
    mhat = rm;
    const float mh = mhat;
#pragma unroll
    for (int r = 0; r < 16; ++r) { pA0[r] = EXP1(pA0[r]); pA1[r] = EXP1(pA1[r]); }
  }

#define STEP(C0, C1, P0, P1, t, ISSUE) do { \
    const lds_cptr kp_ = (lds_cptr)lds + SLOT(IT, 2 * (t) + 9); \
    const lds_cptr vp_ = (lds_cptr)lds + SLOT(IT, 2 * (t) + 10) + c.vrd; \
    kf[0] = KLD(0); kf[1] = KLD(1); \
    ISSUE; \
    float s0 = 0.f, s1 = 0.f; SBAR(); \
    REP16(GAPA, C0, C1, P0, P1); \
    VRD(0); VRD(1); \
    l_reg += s0 + s1; \
    { const float rm = rowmax32(C0, C1) - mhat; resc = false; \
      if (__builtin_expect(__any(rm > THR), 0)) { const float dl = __builtin_fmaxf(rm, 0.f); mhat += dl; \
          fres = __builtin_amdgcn_exp2f(-dl); l_reg *= fres; resc = true; } } \
    const float mh = mhat; SBAR(); \
    REP16(GAPB, C0, C1); \
    if (resc) { _Pragma("unroll") for (int d_ = 0; d_ < 4; ++d_) _Pragma("unroll") for (int r = 0; r < 16; ++r) o[d_][r] *= fres; } \
  } while (0)

  WAITBAR(14);
  STAMP(8 * IT + 3);
  STEP(pB0, pB1, pA0, pA1, 1, DMA_V(Vp, 4, SLOT(IT, 20))); WAITBAR(12);
  for (int t = 2; t < 12; t += 2) {
    STEP(pA0, pA1, pB0, pB1, t,     { DMA_K(Kp, t + 4, SLOT(IT, 2 * t + 17)); DMA_V(Vp, t + 3, SLOT(IT, 2 * t + 18)); }); WAITBAR(12);
    STEP(pB0, pB1, pA0, pA1, t + 1, { DMA_K(Kp, t + 5, SLOT(IT, 2 * t + 19)); DMA_V(Vp, t + 4, SLOT(IT, 2 * t + 20)); }); WAITBAR(12);
  }
  STAMP(8 * IT + 4);
  if constexpr (has_next) {
    STEP(pA0, pA1, pB0, pB1, 12, { DMA_V(Vp, 15, SLOT(IT, 41)); DMA_W(wq_next, 0, SLOT(1, 0)); }); WAITBAR(12);
    STEP(pB0, pB1, pA0, pA1, 13, { DMA_W(wq_next, 1, SLOT(1, 1)); DMA_W(wq_next, 2, SLOT(1, 2)); }); WAITBAR(12);
    STEP(pA0, pA1, pB0, pB1, 14, { DMA_W(wq_next, 3, SLOT(1, 3)); DMA_W(wq_next, 4, SLOT(1, 4)); }); WAITBAR(12);
    STEP(pB0, pB1, pA0, pA1, 15, { DMA_W(wq_next, 5, SLOT(1, 5)); DMA_W(wq_next, 6, SLOT(1, 6)); }); WAITBAR(12);
    DMA_W(wq_next, 7, SLOT(1, 7)); DMA_W(wq_next, 8, SLOT(1, 8));
  } else {
    STEP(pA0, pA1, pB0, pB1, 12, { DMA_V(Vp, 15, SLOT(IT, 41)); }); WAITBAR(10);
    STEP(pB0, pB1, pA0, pA1, 13, {}); WAITBAR(6);
    STEP(pA0, pA1, pB0, pB1, 14, {}); WAITBAR(2);
    STEP(pB0, pB1, pA0, pA1, 15, {}); WAITBAR(0);
  }
  STAMP(8 * IT + 5);
  {
    float s0 = 0.f, s1 = 0.f;
#pragma unroll
    for (int i = 0; i < 16; ++i) { s0 += pB0[i]; s1 += pB1[i]; PACKP(pB0, pB1, i); }
    l_reg += s0 + s1;
    const lds_cptr vp_ = (lds_cptr)lds + SLOT(IT, 41) + c.vrd;
#pragma unroll
    for (int i = 0; i < 16; ++i) {
      VRD(i);
      o[i & 3] = MFMA32(comb(vlo[i % 3], vhi[i % 3]), __builtin_bit_cast(bf16x8, pw[i >> 2]), o[i & 3]);
    }
  }
  if constexpr (has_next) { XLD4(xb0, x_next, 0); XLD4(xb1, x_next, 1); XLD4(xb2, x_next, 2); }
  {
    auto rr = __builtin_amdgcn_permlane32_swap(__float_as_uint(l_reg), __float_as_uint(l_reg), false, false);
    l_reg = __uint_as_float(rr[0]) + __uint_as_float(rr[1]);
  }
  const float rinv = 1.0f / l_reg;
  if (qvalid) {
#pragma unroll
    for (int db = 0; db < 4; ++db) {
#pragma unroll
      for (int g = 0; g < 4; ++g) {
        u32x2 w;
        w[0] = cvtpk(o[db][4 * g + 0] * rinv, o[db][4 * g + 1] * rinv);
        w[1] = cvtpk(o[db][4 * g + 2] * rinv, o[db][4 * g + 3] * rinv);
        *(u32x2*)(Odst + 32 * db + 8 * g) = w;
      }
    }
  }
  STAMP(8 * IT + 6);
#undef KLD
#undef VRD
#undef PACKP
#undef QKM
#undef GAPA
#undef EXP1
#undef GAPB
#undef STEP
}

__global__ __launch_bounds__(512, 2) void attn_kernel(
    const uint16_t* __restrict__ xA, const uint16_t* __restrict__ kA, const uint16_t* __restrict__ vA, const uint16_t* __restrict__ wqA,
    const uint16_t* __restrict__ xB, const uint16_t* __restrict__ kB, const uint16_t* __restrict__ vB, const uint16_t* __restrict__ wqB,
    uint16_t* __restrict__ Ows) {
  __shared__ __attribute__((aligned(16))) char lds[LDS_BYTES];
  const int tid = threadIdx.x, lane = tid & 63, r32 = lane & 31, hi = lane >> 5;
  const int wid = __builtin_amdgcn_readfirstlane(tid >> 6);
  const int bh = blockIdx.x;
  const int h = bh & 31, b = bh >> 5;
  const unsigned lds0 = (unsigned)(uintptr_t)lds;
  unsigned long long* stamp_buf = (unsigned long long*)((char*)Ows + (64u << 20));

  const int row8 = (lane & 31) >> 2, c4 = lane & 3, sub = lane >> 5;
  LaneC c;
  c.vrd = ((lane & 3) << 3) | (((lane >> 2) & 3) << 6) | (((lane >> 4) & 1) << 5) | ((lane >> 5) << 8);
  const int kk = 8 * wid + row8;
  const int kx = (kk & ~12) | ((kk & 4) << 1) | ((kk & 8) >> 1);
  c.woff = (unsigned)((kx * HD + 32 * sub + 8 * c4) * 2);
  {
    const int kr0 = 8 * wid + (lane >> 4), kr1 = kr0 + 4;
    c.koff0 = (unsigned)((kr0 * DH + (((lane & 15) ^ (kr0 & 15)) * 8)) * 2);
    c.koff1 = (unsigned)((kr1 * DH + (((lane & 15) ^ (kr1 & 15)) * 8)) * 2);
    c.voff0 = (unsigned)(((8 * wid + row8) * DH + 32 * sub + 8 * c4) * 2);
    c.voff1 = c.voff0 + 128;
  }
  int qrow = 32 * wid + r32; if (qrow > SQ - 1) qrow = SQ - 1;
  c.xoff = (unsigned)((qrow * DMODEL + 8 * hi) * 2);
#pragma unroll
  for (int d0 = 0; d0 < 8; ++d0) c.kaddr[d0] = r32 * 256 + (((2 * d0 + hi) ^ (r32 & 15)) << 4);
  c.dst0 = lds0 + (2 * wid) * 1024;

  const char* xa = (const char*)uni(xA + (size_t)b * SQ * DMODEL);
  const char* xbp = (const char*)uni(xB + (size_t)b * SQ * DMODEL);
  const char* ka = (const char*)uni(kA + (size_t)(b * NH + h) * SK * DH);
  const char* kb = (const char*)uni(kB + (size_t)(b * NH + h) * SK * DH);
  const char* va = (const char*)uni(vA + (size_t)(b * NH + h) * SK * DH);
  const char* vb = (const char*)uni(vB + (size_t)(b * NH + h) * SK * DH);
  const char* wa = (const char*)uni(wqA + h * DH);
  const char* wb = (const char*)uni(wqB + h * DH);
  const int q = 32 * wid + r32;
  uint16_t* OdA = Ows + ((size_t)b * SQ + q) * HD + h * DH + 4 * hi;
  uint16_t* OdB = OdA + (size_t)MROWS * HD;
  const bool qvalid = q < SQ;

  bf16x8 xb0[4], xb1[4], xb2[4], xb3[4];
#pragma unroll
  for (int j = 0; j < 10; ++j) DMA_W(wa, j, SLOT(0, j));
  XLD4(xb0, xa, 0); XLD4(xb1, xa, 1); XLD4(xb2, xa, 2);
  attn_item<0>(lds, c, wid, hi, xa, ka, va, wa, wb, xbp, xb0, xb1, xb2, xb3, OdA, qvalid, stamp_buf);
  attn_item<1>(lds, c, wid, hi, xbp, kb, vb, wb, wb, xbp, xb0, xb1, xb2, xb3, OdB, qvalid, stamp_buf);
#undef SLOT
#undef DMA_W
#undef DMA_K
#undef DMA_V
#undef XLD
#undef XLD4
#undef WAITX
#undef WAITBAR
}

constexpr int G_BM = 128, G_BN = 64, G_BK = 64;
constexpr int G_AB = G_BM * G_BK * 2, G_BB = G_BK * G_BN * 2, G_STAGE = G_AB + G_BB;

__global__ __launch_bounds__(256) void oproj_kernel(const uint16_t* __restrict__ Ows, const uint16_t* __restrict__ woA,
                                                   const uint16_t* __restrict__ woB, uint16_t* __restrict__ out) {
  __shared__ __attribute__((aligned(16))) char lds[2 * G_STAGE];
  const int tid = threadIdx.x, lane = tid & 63, r32 = lane & 31, hi = lane >> 5;
  const int wid = __builtin_amdgcn_readfirstlane(tid >> 6);
  const int nt = blockIdx.x, mt = blockIdx.y, blk = blockIdx.z;
  const uint16_t* A = Ows + ((size_t)blk * MROWS + (size_t)mt * G_BM) * HD;
  const uint16_t* Bw = (blk ? woB : woA) + nt * G_BN;
  const unsigned lds0 = (unsigned)(uintptr_t)lds;
  const int wm = wid >> 1, wn = wid & 1;
  const uint16_t* asrc[4];
#pragma unroll
  for (int i = 0; i < 4; ++i) {
    const int row = 8 * (4 * wid + i) + (lane >> 3);
    asrc[i] = A + (size_t)row * HD + (((lane & 7) ^ ((row >> 1) & 7)) * 8);
  }
  const int row8 = (lane & 31) >> 2, c4 = lane & 3, sub = lane >> 5;
  const uint16_t* bsrc[2];
#pragma unroll
  for (int i = 0; i < 2; ++i) {
    const int kk = 8 * (2 * wid + i) + row8;
    const int kx = (kk & ~12) | ((kk & 4) << 1) | ((kk & 8) >> 1);
    bsrc[i] = Bw + (size_t)kx * DMODEL + 32 * sub + 8 * c4;
  }
#define G_STG(kt, buf) do { _Pragma("unroll") for (int i = 0; i < 4; ++i) glds16(asrc[i] + (size_t)(kt) * G_BK, RFL(lds0 + (buf) * G_STAGE + (4 * wid + i) * 1024)); \
    _Pragma("unroll") for (int i = 0; i < 2; ++i) glds16(bsrc[i] + (size_t)(kt) * G_BK * DMODEL, RFL(lds0 + (buf) * G_STAGE + G_AB + (2 * wid + i) * 1024)); } while (0)
  const int vrd = ((lane & 3) << 3) | (((lane >> 2) & 3) << 6) | (((lane >> 4) & 1) << 5) | ((lane >> 5) << 8);
  int aaddr[2][4];
#pragma unroll
  for (int mb = 0; mb < 2; ++mb)
#pragma unroll
    for (int ks = 0; ks < 4; ++ks) {
      const int row = 64 * wm + 32 * mb + r32;
      aaddr[mb][ks] = row * 128 + (((2 * ks + hi) ^ ((row >> 1) & 7)) << 4);
    }
  f32x16 acc[2];
  acc[0] = f32x16{}; acc[1] = f32x16{};
  constexpr int NKT = HD / G_BK;
  G_STG(0, 0);
  for (int kt = 0; kt < NKT; ++kt) {
    const int buf = kt & 1;
    asm volatile("s_waitcnt vmcnt(0)\n\ts_barrier" ::: "memory");
    if (kt + 1 < NKT) G_STG(kt + 1, buf ^ 1);
    const lds_cptr ap = (lds_cptr)lds + buf * G_STAGE;
    const lds_cptr bp = (lds_cptr)lds + buf * G_STAGE + G_AB + vrd + wn * 512;
#pragma unroll
    for (int ks = 0; ks < 4; ++ks) {
      s16x4 lo = vtr(bp + ks * 2048), hh = vtr(bp + ks * 2048 + 1024);
      bf16x8 bfr = comb(lo, hh);
#pragma unroll
      for (int mb = 0; mb < 2; ++mb) {
        bf16x8 afr = *(const __attribute__((address_space(3))) bf16x8*)(ap + aaddr[mb][ks]);
        acc[mb] = MFMA32(bfr, afr, acc[mb]);
      }
    }
  }
#undef G_STG
#pragma unroll
  for (int mb = 0; mb < 2; ++mb) {
    const int m = mt * G_BM + 64 * wm + 32 * mb + r32;
    uint16_t* dst = out + (size_t)m * (2 * DMODEL) + blk * DMODEL + nt * G_BN + 32 * wn + 4 * hi;
#pragma unroll
    for (int g = 0; g < 4; ++g) {
      u32x2 w;
      w[0] = cvtpk(acc[mb][4 * g + 0], acc[mb][4 * g + 1]);
      w[1] = cvtpk(acc[mb][4 * g + 2], acc[mb][4 * g + 3]);
      *(u32x2*)(dst + 8 * g) = w;
    }
  }
}

extern "C" void kernel_launch(void* const* d_in, const int* in_sizes, int n_in, void* d_out, int out_size, void* d_ws, size_t ws_size,
                              hipStream_t stream) {
  const uint16_t* xA = (const uint16_t*)d_in[0];
  const uint16_t* kA = (const uint16_t*)d_in[1];
  const uint16_t* vA = (const uint16_t*)d_in[2];
  const uint16_t* wqA = (const uint16_t*)d_in[3];
  const uint16_t* woA = (const uint16_t*)d_in[4];
  const uint16_t* xB = (const uint16_t*)d_in[5];
  const uint16_t* kB = (const uint16_t*)d_in[6];
  const uint16_t* vB = (const uint16_t*)d_in[7];
  const uint16_t* wqB = (const uint16_t*)d_in[8];
  const uint16_t* woB = (const uint16_t*)d_in[9];
  uint16_t* Ows = (uint16_t*)d_ws;
  attn_kernel<<<dim3(256), dim3(512), 0, stream>>>(xA, kA, vA, wqA, xB, kB, vB, wqB, Ows);
  oproj_kernel<<<dim3(DMODEL / G_BN, MROWS / G_BM, 2), dim3(256), 0, stream>>>(Ows, woA, woB, (uint16_t*)d_out);
}
```

```cpp
#include <hip/hip_runtime.h>
#include <hip/hip_bf16.h>
#include <stdint.h>

typedef __attribute__((ext_vector_type(8))) short bf16x8;
typedef __attribute__((ext_vector_type(4))) short s16x4;
typedef __attribute__((ext_vector_type(16))) float f32x16;
typedef __attribute__((ext_vector_type(4))) unsigned u32x4;
typedef __attribute__((ext_vector_type(2))) unsigned u32x2;
typedef float f32x2_t __attribute__((ext_vector_type(2)));
typedef __bf16 bf16x2_t __attribute__((ext_vector_type(2)));
typedef __attribute__((address_space(3))) const char* lds_cptr;
typedef short v4i16_t __attribute__((ext_vector_type(4)));

#define DI __device__ __forceinline__
#define SBAR() __builtin_amdgcn_sched_barrier(0)
#define PIN(x) asm volatile("" : "+v"(x))
#define MFMA32(a, b, c) __builtin_amdgcn_mfma_f32_32x32x16_bf16((a), (b), (c), 0, 0, 0)
#define RFL(x) ((unsigned)__builtin_amdgcn_readfirstlane((int)(x)))

constexpr int NBATCH = 8, SQ = 240, DMODEL = 640, NH = 32, SK = 1024, DH = 128;
constexpr int MROWS = NBATCH * SQ;
constexpr int HD = NH * DH;
constexpr float QSCALE = 0.08838834764831845f * 1.4426950408889634f;

#define STAMP(i) do {} while (0)
#define TNOW(t_) do {} while (0)
DI int crow(int r, int hi) { return (r & 3) + 8 * (r >> 2) + 4 * hi; }
DI unsigned cvtpk(float lo, float hi) {
  f32x2_t v = {lo, hi};
  bf16x2_t b = __builtin_convertvector(v, bf16x2_t);
  return __builtin_bit_cast(unsigned, b);
}
DI void glds16(const void* g, unsigned lds_base) {
  unsigned sv;
  asm volatile("s_mov_b32 %0, m0\n\ts_mov_b32 m0, %2\n\ts_nop 0\n\tglobal_load_lds_dwordx4 %1, off\n\ts_mov_b32 m0, %0"
               : "=&s"(sv) : "v"(g), "s"(lds_base) : "memory");
}
DI s16x4 vtr(lds_cptr p) {
  return __builtin_bit_cast(s16x4, __builtin_amdgcn_ds_read_tr16_b64_v4i16((__attribute__((address_space(3))) v4i16_t*)p));
}
DI bf16x8 comb(s16x4 lo, s16x4 hi) { return (bf16x8){lo[0], lo[1], lo[2], lo[3], hi[0], hi[1], hi[2], hi[3]}; }
#define MX3(a, b, c) __builtin_fmaxf(__builtin_fmaxf((a), (b)), (c))
DI float rowmax32(const f32x16& p0, const f32x16& p1) {
  float a = MX3(p0[0], p0[1], p1[0]), b = MX3(p0[2], p0[3], p1[1]);
  a = MX3(a, p1[2], p1[3]);
#pragma unroll
  for (int r = 4; r < 16; r += 4) {
    a = MX3(a, p0[r], p0[r + 1]); b = MX3(b, p0[r + 2], p0[r + 3]);
    a = MX3(a, p1[r], p1[r + 1]); b = MX3(b, p1[r + 2], p1[r + 3]);
  }
  float m = __builtin_fmaxf(a, b);
  auto rr = __builtin_amdgcn_permlane32_swap(__float_as_uint(m), __float_as_uint(m), false, false);
  return __builtin_fmaxf(__uint_as_float(rr[0]), __uint_as_float(rr[1]));
}

constexpr int NSLOT = 10, SLOTB = 16384, LDS_BYTES = NSLOT * SLOTB;
constexpr float THR = 8.0f;

DI void glds16s(unsigned voff, const void* sbase, unsigned lds_base) {
  unsigned sv;
  asm volatile("s_mov_b32 %0, m0\n\ts_mov_b32 m0, %3\n\ts_nop 0\n\tglobal_load_lds_dwordx4 %1, %2\n\ts_mov_b32 m0, %0"
               : "=&s"(sv) : "v"(voff), "s"(sbase), "s"(lds_base) : "memory");
}
DI const void* uni(const void* p) {
  const unsigned long long v = (unsigned long long)p;
  const unsigned lo = RFL((unsigned)v), hi = RFL((unsigned)(v >> 32));
  return (const void*)(((unsigned long long)hi << 32) | lo);
}

struct LaneC {
  unsigned woff, koff0, koff1, voff0, voff1, dst0, xdst;
  unsigned xsrc[4];
  int kaddr[8];
  int xaddr[4];
  int vrd;
};

#define SLOT(it_, n_) ((unsigned)(((2 * (it_) + (n_)) % NSLOT) * SLOTB))
#define DMA_W(W_, j, sl) do { glds16s(c.woff, (W_) + (size_t)(j) * 64 * HD * 2, RFL(c.dst0 + (sl))); glds16s(c.woff + 128, (W_) + (size_t)(j) * 64 * HD * 2, RFL(c.dst0 + (sl) + 1024)); } while (0)
#define DMA_K(K_, t, sl) do { glds16s(c.koff0, (K_) + (size_t)(t) * 64 * DH * 2, RFL(c.dst0 + (sl))); glds16s(c.koff1, (K_) + (size_t)(t) * 64 * DH * 2, RFL(c.dst0 + (sl) + 1024)); } while (0)
#define DMA_V(V_, t, sl) do { glds16s(c.voff0, (V_) + (size_t)(t) * 64 * DH * 2, RFL(c.dst0 + (sl))); glds16s(c.voff1, (V_) + (size_t)(t) * 64 * DH * 2, RFL(c.dst0 + (sl) + 1024)); } while (0)
#define DMA_X(X_, j, slA, slB) do { const unsigned d_ = RFL(c.xdst + ((wid < 4) ? (slA) : (slB))); \
    glds16s(c.xsrc[0], (X_) + 128 * (j), d_); glds16s(c.xsrc[1], (X_) + 128 * (j), d_ + 1024); \
    glds16s(c.xsrc[2], (X_) + 128 * (j), d_ + 2048); glds16s(c.xsrc[3], (X_) + 128 * (j), d_ + 3072); } while (0)
#define DMA_WX(W_, X_, j, it_) do { DMA_W(W_, j, SLOT(it_, 3 * (j))); DMA_X(X_, j, SLOT(it_, 3 * (j) + 1), SLOT(it_, 3 * (j) + 2)); } while (0)
#define WAITBAR(N) asm volatile("s_waitcnt vmcnt(" #N ")\n\ts_barrier" ::: "memory")
#define REP16(M, ...) M(__VA_ARGS__, 0); M(__VA_ARGS__, 1); M(__VA_ARGS__, 2); M(__VA_ARGS__, 3); M(__VA_ARGS__, 4); M(__VA_ARGS__, 5); M(__VA_ARGS__, 6); M(__VA_ARGS__, 7); \
                      M(__VA_ARGS__, 8); M(__VA_ARGS__, 9); M(__VA_ARGS__, 10); M(__VA_ARGS__, 11); M(__VA_ARGS__, 12); M(__VA_ARGS__, 13); M(__VA_ARGS__, 14); M(__VA_ARGS__, 15)

template <int IT>
DI void attn_item(const char* lds, const LaneC& c, const int wid, const int hi,
                  const char* x, const char* Kp, const char* Vp, const char* wq_cur, const char* wq_next, const char* x_next,
                  uint16_t* __restrict__ Odst, const bool qvalid, unsigned long long* stamp_buf) {
  constexpr bool has_next = (IT == 0);
  STAMP(8 * IT + 0);
  s16x4 vlo[3], vhi[3];
#define VRD(i) do { vlo[(i) % 3] = vtr(vp_ + ((i) >> 2) * 4096 + ((i) & 3) * 512); vhi[(i) % 3] = vtr(vp_ + ((i) >> 2) * 4096 + 2048 + ((i) & 3) * 512); } while (0)
  bf16x8 qr[8];
  {
    f32x16 qa[4];
#pragma unroll
    for (int i = 0; i < 4; ++i) qa[i] = f32x16{};
    bf16x8 xf[4];
#define QGAP(i) do { if ((i) + 2 < 16) VRD((i) + 2); SBAR(); \
      qa[(i) & 3] = MFMA32(comb(vlo[(i) % 3], vhi[(i) % 3]), xf[(i) >> 2], qa[(i) & 3]); SBAR(); } while (0)
#define QGAP1(dummy, i) QGAP(i)
#define QSTEP(j, NW, ISSUE) do { \
      WAITBAR(NW); \
      const lds_cptr vp_ = (lds_cptr)lds + SLOT(IT, 3 * (j)) + c.vrd; \
      const lds_cptr xp_ = (lds_cptr)lds + ((wid < 4) ? SLOT(IT, 3 * (j) + 1) : SLOT(IT, 3 * (j) + 2)); \
      VRD(0); xf[0] = *(const __attribute__((address_space(3))) bf16x8*)(xp_ + c.xaddr[0]); VRD(1); \
      _Pragma("unroll") for (int ks = 1; ks < 4; ++ks) xf[ks] = *(const __attribute__((address_space(3))) bf16x8*)(xp_ + c.xaddr[ks]); \
      ISSUE; SBAR(); \
      REP16(QGAP1, 0); \
    } while (0)
    if constexpr (IT == 0) { QSTEP(0, 14, {}); } else { QSTEP(0, 8, DMA_W(wq_cur, 3, SLOT(IT, 9))); }
    QSTEP(1, 8, { DMA_X(x, 3, SLOT(IT, 10), SLOT(IT, 11)); DMA_W(wq_cur, 4, SLOT(IT, 12)); });
    QSTEP(2, 8, { DMA_X(x, 4, SLOT(IT, 13), SLOT(IT, 14)); DMA_W(wq_cur, 5, SLOT(IT, 15)); });
    QSTEP(3, 8, { DMA_X(x, 5, SLOT(IT, 16), SLOT(IT, 17)); DMA_W(wq_cur, 6, SLOT(IT, 18)); });
    QSTEP(4, 8, { DMA_X(x, 6, SLOT(IT, 19), SLOT(IT, 20)); DMA_W(wq_cur, 7, SLOT(IT, 21)); });
    QSTEP(5, 8, { DMA_X(x, 7, SLOT(IT, 22), SLOT(IT, 23)); DMA_W(wq_cur, 8, SLOT(IT, 24)); });
    QSTEP(6, 8, { DMA_X(x, 8, SLOT(IT, 25), SLOT(IT, 26)); DMA_W(wq_cur, 9, SLOT(IT, 27)); });
    QSTEP(7, 8, { DMA_X(x, 9, SLOT(IT, 28), SLOT(IT, 29)); DMA_K(Kp, 0, SLOT(IT, 30)); });
    QSTEP(8, 8, { DMA_K(Kp, 1, SLOT(IT, 31)); DMA_V(Vp, 0, SLOT(IT, 32)); DMA_K(Kp, 2, SLOT(IT, 33)); });
    QSTEP(9, 8, { DMA_V(Vp, 1, SLOT(IT, 34)); DMA_K(Kp, 3, SLOT(IT, 35)); DMA_V(Vp, 2, SLOT(IT, 36)); });
#undef QSTEP
#undef QGAP
#undef QGAP1
#pragma unroll
    for (int t = 0; t < 4; ++t) {
#pragma unroll
      for (int e = 0; e < 2; ++e) {
        const int B0 = 8 * e;
        unsigned a0 = cvtpk(qa[t][B0 + 0] * QSCALE, qa[t][B0 + 1] * QSCALE), a1 = cvtpk(qa[t][B0 + 2] * QSCALE, qa[t][B0 + 3] * QSCALE);
        unsigned b0 = cvtpk(qa[t][B0 + 4] * QSCALE, qa[t][B0 + 5] * QSCALE), b1 = cvtpk(qa[t][B0 + 6] * QSCALE, qa[t][B0 + 7] * QSCALE);
        auto r0 = __builtin_amdgcn_permlane32_swap(a0, b0, false, false);
        auto r1 = __builtin_amdgcn_permlane32_swap(a1, b1, false, false);
        u32x4 w = {r0[0], r1[0], r0[1], r1[1]};
        qr[2 * t + e] = __builtin_bit_cast(bf16x8, w);
      }
    }
  }
  STAMP(8 * IT + 1);

  f32x16 o[4];
#pragma unroll
  for (int i = 0; i < 4; ++i) o[i] = f32x16{};
  float mhat = 0.f, l_reg = 0.f, fres = 1.f;
  bool resc = false;
  f32x16 pA0, pA1, pB0, pB1;
  bf16x8 kf[3];
  u32x4 pw[4];

#define KLD(i) (*(const __attribute__((address_space(3))) bf16x8*)(kp_ + c.kaddr[(i) >> 1] + ((i) & 1) * 8192))
#define PACKP(P0, P1, i) do { if ((i) < 8) pw[(i) >> 2][(i) & 3] = cvtpk(P0[2 * (i)], P0[2 * (i) + 1]); \
                              else pw[2 + (((i) - 8) >> 2)][((i) - 8) & 3] = cvtpk(P1[2 * ((i) - 8)], P1[2 * ((i) - 8) + 1]); } while (0)
#define QKM(C0, C1, i) do { if ((i) & 1) { C1 = MFMA32(kf[(i) % 3], qr[(i) >> 1], ((i) < 2) ? f32x16{} : C1); } \
                            else { C0 = MFMA32(kf[(i) % 3], qr[(i) >> 1], ((i) < 2) ? f32x16{} : C0); } } while (0)
#define GAPA(C0, C1, P0, P1, i) do { if ((i) + 2 < 16) kf[((i) + 2) % 3] = KLD((i) + 2); SBAR(); \
    QKM(C0, C1, i); s0 += P0[i]; s1 += P1[i]; PACKP(P0, P1, i); PIN(pw[(i) >> 2]); PIN(s0); PIN(s1); SBAR(); } while (0)
#define EXP1(v) __builtin_amdgcn_exp2f((v) - mh)
#define GAPB(C0, C1, i) do { if ((i) + 2 < 16) VRD((i) + 2); SBAR(); \
    o[(i) & 3] = MFMA32(comb(vlo[(i) % 3], vhi[(i) % 3]), __builtin_bit_cast(bf16x8, pw[(i) >> 2]), o[(i) & 3]); \
    if ((i) < 8) { C0[2 * (i)] = EXP1(C0[2 * (i)]); C0[2 * (i) + 1] = EXP1(C0[2 * (i) + 1]); PIN(C0); } \
    else { C1[2 * ((i) - 8)] = EXP1(C1[2 * ((i) - 8)]); C1[2 * ((i) - 8) + 1] = EXP1(C1[2 * ((i) - 8) + 1]); PIN(C1); } SBAR(); } while (0)

  WAITBAR(12);
  DMA_K(Kp, 4, SLOT(IT, 37)); DMA_V(Vp, 3, SLOT(IT, 38)); DMA_K(Kp, 5, SLOT(IT, 39));
  STAMP(8 * IT + 2);
  {
    const lds_cptr kp_ = (lds_cptr)lds + SLOT(IT, 30);
#pragma unroll
    for (int i = 0; i < 16; ++i) { kf[i % 3] = KLD(i); QKM(pA0, pA1, i); }
    const float rm = rowmax32(pA0, pA1);
    mhat = rm;
    const float mh = mhat;
#pragma unroll
    for (int r = 0; r < 16; ++r) { pA0[r] = EXP1(pA0[r]); pA1[r] = EXP1(pA1[r]); }
  }

#define STEP(C0, C1, P0, P1, t, ISSUE) do { \
    const lds_cptr kp_ = (lds_cptr)lds + SLOT(IT, 2 * (t) + 29); \
    const lds_cptr vp_ = (lds_cptr)lds + SLOT(IT, 2 * (t) + 30) + c.vrd; \
    kf[0] = KLD(0); kf[1] = KLD(1); \
    ISSUE; \
    float s0 = 0.f, s1 = 0.f; SBAR(); \
    REP16(GAPA, C0, C1, P0, P1); \
    VRD(0); VRD(1); \
    l_reg += s0 + s1; \
    { const float rm = rowmax32(C0, C1) - mhat; resc = false; \
      if (__builtin_expect(__any(rm > THR), 0)) { const float dl = __builtin_fmaxf(rm, 0.f); mhat += dl; \
          fres = __builtin_amdgcn_exp2f(-dl); l_reg *= fres; resc = true; } } \
    const float mh = mhat; SBAR(); \
    REP16(GAPB, C0, C1); \
    if (resc) { _Pragma("unroll") for (int d_ = 0; d_ < 4; ++d_) _Pragma("unroll") for (int r = 0; r < 16; ++r) o[d_][r] *= fres; } \
  } while (0)

  WAITBAR(14);
  STAMP(8 * IT + 3);
  STEP(pB0, pB1, pA0, pA1, 1, DMA_V(Vp, 4, SLOT(IT, 40))); WAITBAR(12);
  for (int t = 2; t < 12; t += 2) {
    STEP(pA0, pA1, pB0, pB1, t,     { DMA_K(Kp, t + 4, SLOT(IT, 2 * t + 37)); DMA_V(Vp, t + 3, SLOT(IT, 2 * t + 38)); }); WAITBAR(12);
    STEP(pB0, pB1, pA0, pA1, t + 1, { DMA_K(Kp, t + 5, SLOT(IT, 2 * t + 39)); DMA_V(Vp, t + 4, SLOT(IT, 2 * t + 40)); }); WAITBAR(12);
  }
  STAMP(8 * IT + 4);
  if constexpr (has_next) {
    STEP(pA0, pA1, pB0, pB1, 12, { DMA_V(Vp, 15, SLOT(IT, 61)); DMA_W(wq_next, 0, SLOT(1, 0)); }); WAITBAR(12);
    STEP(pB0, pB1, pA0, pA1, 13, { DMA_X(x_next, 0, SLOT(1, 1), SLOT(1, 2)); }); WAITBAR(12);
    STEP(pA0, pA1, pB0, pB1, 14, { DMA_W(wq_next, 1, SLOT(1, 3)); if (wid < 4) DMA_X(x_next, 1, SLOT(1, 4), SLOT(1, 4)); });
    if (wid < 4) WAITBAR(12); else WAITBAR(8);
    STEP(pB0, pB1, pA0, pA1, 15, { if (wid >= 4) DMA_X(x_next, 1, SLOT(1, 5), SLOT(1, 5)); DMA_W(wq_next, 2, SLOT(1, 6)); });
    if (wid < 4) WAITBAR(8); else WAITBAR(12);
    DMA_X(x_next, 2, SLOT(1, 7), SLOT(1, 8));
  } else {
    STEP(pA0, pA1, pB0, pB1, 12, { DMA_V(Vp, 15, SLOT(IT, 61)); }); WAITBAR(10);
    STEP(pB0, pB1, pA0, pA1, 13, {}); WAITBAR(6);
    STEP(pA0, pA1, pB0, pB1, 14, {}); WAITBAR(2);
    STEP(pB0, pB1, pA0, pA1, 15, {}); WAITBAR(0);
  }
  STAMP(8 * IT + 5);
  {
    float s0 = 0.f, s1 = 0.f;
#pragma unroll
    for (int i = 0; i < 16; ++i) { s0 += pB0[i]; s1 += pB1[i]; PACKP(pB0, pB1, i); }
    l_reg += s0 + s1;
    const lds_cptr vp_ = (lds_cptr)lds + SLOT(IT, 61) + c.vrd;
#pragma unroll
    for (int i = 0; i < 16; ++i) {
      VRD(i);
      o[i & 3] = MFMA32(comb(vlo[i % 3], vhi[i % 3]), __builtin_bit_cast(bf16x8, pw[i >> 2]), o[i & 3]);
    }
  }
  {
    auto rr = __builtin_amdgcn_permlane32_swap(__float_as_uint(l_reg), __float_as_uint(l_reg), false, false);
    l_reg = __uint_as_float(rr[0]) + __uint_as_float(rr[1]);
  }
  const float rinv = 1.0f / l_reg;
  if (qvalid) {
#pragma unroll
    for (int db = 0; db < 4; ++db) {
#pragma unroll
      for (int g = 0; g < 4; ++g) {
        u32x2 w;
        w[0] = cvtpk(o[db][4 * g + 0] * rinv, o[db][4 * g + 1] * rinv);
        w[1] = cvtpk(o[db][4 * g + 2] * rinv, o[db][4 * g + 3] * rinv);
        *(u32x2*)(Odst + 32 * db + 8 * g) = w;
      }
    }
  }
  STAMP(8 * IT + 6);
#undef KLD
#undef VRD
#undef PACKP
#undef QKM
#undef GAPA
#undef EXP1
#undef GAPB
#undef STEP
}

__global__ __launch_bounds__(512, 2) void attn_kernel(
    const uint16_t* __restrict__ xA, const uint16_t* __restrict__ kA, const uint16_t* __restrict__ vA, const uint16_t* __restrict__ wqA,
    const uint16_t* __restrict__ xB, const uint16_t* __restrict__ kB, const uint16_t* __restrict__ vB, const uint16_t* __restrict__ wqB,
    uint16_t* __restrict__ Ows) {
  __shared__ __attribute__((aligned(16))) char lds[LDS_BYTES];
  const int tid = threadIdx.x, lane = tid & 63, r32 = lane & 31, hi = lane >> 5;
  const int wid = __builtin_amdgcn_readfirstlane(tid >> 6);
  const int bh = blockIdx.x;
  const int h = bh & 31, b = bh >> 5;
  const unsigned lds0 = (unsigned)(uintptr_t)lds;
  unsigned long long* stamp_buf = (unsigned long long*)((char*)Ows + (64u << 20));

  const int row8 = (lane & 31) >> 2, c4 = lane & 3, sub = lane >> 5;
  LaneC c;
  c.vrd = ((lane & 3) << 3) | (((lane >> 2) & 3) << 6) | (((lane >> 4) & 1) << 5) | ((lane >> 5) << 8);
  const int kk = 8 * wid + row8;
  const int kx = (kk & ~12) | ((kk & 4) << 1) | ((kk & 8) >> 1);
  c.woff = (unsigned)((kx * HD + 32 * sub + 8 * c4) * 2);
  {
    const int kr0 = 8 * wid + (lane >> 4), kr1 = kr0 + 4;
    c.koff0 = (unsigned)((kr0 * DH + (((lane & 15) ^ (kr0 & 15)) * 8)) * 2);
    c.koff1 = (unsigned)((kr1 * DH + (((lane & 15) ^ (kr1 & 15)) * 8)) * 2);
    c.voff0 = (unsigned)(((8 * wid + row8) * DH + 32 * sub + 8 * c4) * 2);
    c.voff1 = c.voff0 + 128;
  }
#pragma unroll
  for (int i = 0; i < 4; ++i) {
    const int row = 32 * wid + 8 * i + (lane >> 3);
    const int rc = row > SQ - 1 ? SQ - 1 : row;
    c.xsrc[i] = (unsigned)(rc * DMODEL * 2 + (((lane & 7) ^ ((row >> 1) & 7)) << 4));
    const int xr = 32 * wid + r32;
    c.xaddr[i] = (xr & 127) * 128 + (((2 * i + hi) ^ ((xr >> 1) & 7)) << 4);
  }
#pragma unroll
  for (int d0 = 0; d0 < 8; ++d0) c.kaddr[d0] = r32 * 256 + (((2 * d0 + hi) ^ (r32 & 15)) << 4);
  c.dst0 = lds0 + (2 * wid) * 1024;
  c.xdst = lds0 + (wid & 3) * 4096;

  const char* xa = (const char*)uni(xA + (size_t)b * SQ * DMODEL);
  const char* xbp = (const char*)uni(xB + (size_t)b * SQ * DMODEL);
  const char* ka = (const char*)uni(kA + (size_t)(b * NH + h) * SK * DH);
  const char* kb = (const char*)uni(kB + (size_t)(b * NH + h) * SK * DH);
  const char* va = (const char*)uni(vA + (size_t)(b * NH + h) * SK * DH);
  const char* vb = (const char*)uni(vB + (size_t)(b * NH + h) * SK * DH);
  const char* wa = (const char*)uni(wqA + h * DH);
  const char* wb = (const char*)uni(wqB + h * DH);
  const int q = 32 * wid + r32;
  uint16_t* OdA = Ows + ((size_t)b * SQ + q) * HD + h * DH + 4 * hi;
  uint16_t* OdB = OdA + (size_t)MROWS * HD;
  const bool qvalid = q < SQ;

  DMA_WX(wa, xa, 0, 0); DMA_WX(wa, xa, 1, 0); DMA_WX(wa, xa, 2, 0); DMA_W(wa, 3, SLOT(0, 9));
  attn_item<0>(lds, c, wid, hi, xa, ka, va, wa, wb, xbp, OdA, qvalid, stamp_buf);
  attn_item<1>(lds, c, wid, hi, xbp, kb, vb, wb, wb, xbp, OdB, qvalid, stamp_buf);
}

constexpr int G_BM = 128, G_BN = 64, G_BK = 64;
constexpr int G_AB = G_BM * G_BK * 2, G_BB = G_BK * G_BN * 2, G_STAGE = G_AB + G_BB;

__global__ __launch_bounds__(256) void oproj_kernel(const uint16_t* __restrict__ Ows, const uint16_t* __restrict__ woA,
                                                   const uint16_t* __restrict__ woB, uint16_t* __restrict__ out) {
  __shared__ __attribute__((aligned(16))) char lds[2 * G_STAGE];
  const int tid = threadIdx.x, lane = tid & 63, r32 = lane & 31, hi = lane >> 5;
  const int wid = __builtin_amdgcn_readfirstlane(tid >> 6);
  const int nt = blockIdx.x, mt = blockIdx.y, blk = blockIdx.z;
  const uint16_t* A = Ows + ((size_t)blk * MROWS + (size_t)mt * G_BM) * HD;
  const uint16_t* Bw = (blk ? woB : woA) + nt * G_BN;
  const unsigned lds0 = (unsigned)(uintptr_t)lds;
  const int wm = wid >> 1, wn = wid & 1;
  const uint16_t* asrc[4];
#pragma unroll
  for (int i = 0; i < 4; ++i) {
    const int row = 8 * (4 * wid + i) + (lane >> 3);
    asrc[i] = A + (size_t)row * HD + (((lane & 7) ^ ((row >> 1) & 7)) * 8);
  }
  const int row8 = (lane & 31) >> 2, c4 = lane & 3, sub = lane >> 5;
  const uint16_t* bsrc[2];
#pragma unroll
  for (int i = 0; i < 2; ++i) {
    const int kk = 8 * (2 * wid + i) + row8;
    const int kx = (kk & ~12) | ((kk & 4) << 1) | ((kk & 8) >> 1);
    bsrc[i] = Bw + (size_t)kx * DMODEL + 32 * sub + 8 * c4;
  }
#define G_STG(kt, buf) do { _Pragma("unroll") for (int i = 0; i < 4; ++i) glds16(asrc[i] + (size_t)(kt) * G_BK, RFL(lds0 + (buf) * G_STAGE + (4 * wid + i) * 1024)); \
    _Pragma("unroll") for (int i = 0; i < 2; ++i) glds16(bsrc[i] + (size_t)(kt) * G_BK * DMODEL, RFL(lds0 + (buf) * G_STAGE + G_AB + (2 * wid + i) * 1024)); } while (0)
  const int vrd = ((lane & 3) << 3) | (((lane >> 2) & 3) << 6) | (((lane >> 4) & 1) << 5) | ((lane >> 5) << 8);
  int aaddr[2][4];
#pragma unroll
  for (int mb = 0; mb < 2; ++mb)
#pragma unroll
    for (int ks = 0; ks < 4; ++ks) {
      const int row = 64 * wm + 32 * mb + r32;
      aaddr[mb][ks] = row * 128 + (((2 * ks + hi) ^ ((row >> 1) & 7)) << 4);
    }
  f32x16 acc[2];
  acc[0] = f32x16{}; acc[1] = f32x16{};
  constexpr int NKT = HD / G_BK;
  G_STG(0, 0);
  for (int kt = 0; kt < NKT; ++kt) {
    const int buf = kt & 1;
    asm volatile("s_waitcnt vmcnt(0)\n\ts_barrier" ::: "memory");
    if (kt + 1 < NKT) G_STG(kt + 1, buf ^ 1);
    const lds_cptr ap = (lds_cptr)lds + buf * G_STAGE;
    const lds_cptr bp = (lds_cptr)lds + buf * G_STAGE + G_AB + vrd + wn * 512;
#pragma unroll
    for (int ks = 0; ks < 4; ++ks) {
      s16x4 lo = vtr(bp + ks * 2048), hh = vtr(bp + ks * 2048 + 1024);
      bf16x8 bfr = comb(lo, hh);
#pragma unroll
      for (int mb = 0; mb < 2; ++mb) {
        bf16x8 afr = *(const __attribute__((address_space(3))) bf16x8*)(ap + aaddr[mb][ks]);
        acc[mb] = MFMA32(bfr, afr, acc[mb]);
      }
    }
  }
#undef G_STG
#pragma unroll
  for (int mb = 0; mb < 2; ++mb) {
    const int m = mt * G_BM + 64 * wm + 32 * mb + r32;
    uint16_t* dst = out + (size_t)m * (2 * DMODEL) + blk * DMODEL + nt * G_BN + 32 * wn + 4 * hi;
#pragma unroll
    for (int g = 0; g < 4; ++g) {
      u32x2 w;
      w[0] = cvtpk(acc[mb][4 * g + 0], acc[mb][4 * g + 1]);
      w[1] = cvtpk(acc[mb][4 * g + 2], acc[mb][4 * g + 3]);
      *(u32x2*)(dst + 8 * g) = w;
    }
  }
}

extern "C" void kernel_launch(void* const* d_in, const int* in_sizes, int n_in, void* d_out, int out_size, void* d_ws, size_t ws_size,
                              hipStream_t stream) {
  const uint16_t* xA = (const uint16_t*)d_in[0];
  const uint16_t* kA = (const uint16_t*)d_in[1];
  const uint16_t* vA = (const uint16_t*)d_in[2];
  const uint16_t* wqA = (const uint16_t*)d_in[3];
  const uint16_t* woA = (const uint16_t*)d_in[4];
  const uint16_t* xB = (const uint16_t*)d_in[5];
  const uint16_t* kB = (const uint16_t*)d_in[6];
  const uint16_t* vB = (const uint16_t*)d_in[7];
  const uint16_t* wqB = (const uint16_t*)d_in[8];
  const uint16_t* woB = (const uint16_t*)d_in[9];
  uint16_t* Ows = (uint16_t*)d_ws;
  attn_kernel<<<dim3(256), dim3(512), 0, stream>>>(xA, kA, vA, wqA, xB, kB, vB, wqB, Ows);
  oproj_kernel<<<dim3(DMODEL / G_BN, MROWS / G_BM, 2), dim3(256), 0, stream>>>(Ows, woA, woB, (uint16_t*)d_out);
}
```

```cpp
#include <hip/hip_runtime.h>
#include <hip/hip_bf16.h>
#include <stdint.h>

typedef __attribute__((ext_vector_type(8))) short bf16x8;
typedef __attribute__((ext_vector_type(4))) short s16x4;
typedef __attribute__((ext_vector_type(16))) float f32x16;
typedef __attribute__((ext_vector_type(4))) unsigned u32x4;
typedef __attribute__((ext_vector_type(2))) unsigned u32x2;
typedef float f32x2_t __attribute__((ext_vector_type(2)));
typedef __bf16 bf16x2_t __attribute__((ext_vector_type(2)));
typedef __attribute__((address_space(3))) const char* lds_cptr;
typedef short v4i16_t __attribute__((ext_vector_type(4)));

#define DI __device__ __forceinline__
#define SBAR() __builtin_amdgcn_sched_barrier(0)
#define PIN(x) asm volatile("" : "+v"(x))
#define MFMA32(a, b, c) __builtin_amdgcn_mfma_f32_32x32x16_bf16((a), (b), (c), 0, 0, 0)
#define RFL(x) ((unsigned)__builtin_amdgcn_readfirstlane((int)(x)))

constexpr int NBATCH = 8, SQ = 240, DMODEL = 640, NH = 32, SK = 1024, DH = 128;
constexpr int MROWS = NBATCH * SQ;
constexpr int HD = NH * DH;
constexpr float QSCALE = 0.08838834764831845f * 1.4426950408889634f;

#define STAMP(i) do {} while (0)
#define TNOW(t_) do {} while (0)
DI int crow(int r, int hi) { return (r & 3) + 8 * (r >> 2) + 4 * hi; }
DI unsigned cvtpk(float lo, float hi) {
  f32x2_t v = {lo, hi};
  bf16x2_t b = __builtin_convertvector(v, bf16x2_t);
  return __builtin_bit_cast(unsigned, b);
}
DI void glds16(const void* g, unsigned lds_base) {
  unsigned sv;
  asm volatile("s_mov_b32 %0, m0\n\ts_mov_b32 m0, %2\n\ts_nop 0\n\tglobal_load_lds_dwordx4 %1, off\n\ts_mov_b32 m0, %0"
               : "=&s"(sv) : "v"(g), "s"(lds_base) : "memory");
}
DI s16x4 vtr(lds_cptr p) {
  return __builtin_bit_cast(s16x4, __builtin_amdgcn_ds_read_tr16_b64_v4i16((__attribute__((address_space(3))) v4i16_t*)p));
}
DI bf16x8 comb(s16x4 lo, s16x4 hi) { return (bf16x8){lo[0], lo[1], lo[2], lo[3], hi[0], hi[1], hi[2], hi[3]}; }
#define MX3(a, b, c) __builtin_fmaxf(__builtin_fmaxf((a), (b)), (c))
DI float rowmax32(const f32x16& p0, const f32x16& p1) {
  float a = MX3(p0[0], p0[1], p1[0]), b = MX3(p0[2], p0[3], p1[1]);
  a = MX3(a, p1[2], p1[3]);
#pragma unroll
  for (int r = 4; r < 16; r += 4) {
    a = MX3(a, p0[r], p0[r + 1]); b = MX3(b, p0[r + 2], p0[r + 3]);
    a = MX3(a, p1[r], p1[r + 1]); b = MX3(b, p1[r + 2], p1[r + 3]);
  }
  float m = __builtin_fmaxf(a, b);
  auto rr = __builtin_amdgcn_permlane32_swap(__float_as_uint(m), __float_as_uint(m), false, false);
  return __builtin_fmaxf(__uint_as_float(rr[0]), __uint_as_float(rr[1]));
}

constexpr int NSLOT = 10, SLOTB = 16384, LDS_BYTES = NSLOT * SLOTB;
constexpr float THR = 8.0f;

DI void glds16s(unsigned voff, const void* sbase, unsigned lds_base) {
  unsigned sv;
  asm volatile("s_mov_b32 %0, m0\n\ts_mov_b32 m0, %3\n\ts_nop 0\n\tglobal_load_lds_dwordx4 %1, %2\n\ts_mov_b32 m0, %0"
               : "=&s"(sv) : "v"(voff), "s"(sbase), "s"(lds_base) : "memory");
}
DI const void* uni(const void* p) {
  const unsigned long long v = (unsigned long long)p;
  const unsigned lo = RFL((unsigned)v), hi = RFL((unsigned)(v >> 32));
  return (const void*)(((unsigned long long)hi << 32) | lo);
}

struct LaneC {
  unsigned woff, koff0, koff1, voff0, voff1, dst0, xdst;
  unsigned xsrc[4];
  int kaddr[8];
  int xaddr[4];
  int vrd;
};

#define SLOT(it_, n_) ((unsigned)(((2 * (it_) + (n_)) % NSLOT) * SLOTB))
#define DMA_W(W_, j, sl) do { glds16s(c.woff, (W_) + (size_t)(j) * 64 * HD * 2, RFL(c.dst0 + (sl))); glds16s(c.woff + 128, (W_) + (size_t)(j) * 64 * HD * 2, RFL(c.dst0 + (sl) + 1024)); } while (0)
#define DMA_K(K_, t, sl) do { glds16s(c.koff0, (K_) + (size_t)(t) * 64 * DH * 2, RFL(c.dst0 + (sl))); glds16s(c.koff1, (K_) + (size_t)(t) * 64 * DH * 2, RFL(c.dst0 + (sl) + 1024)); } while (0)
#define DMA_V(V_, t, sl) do { glds16s(c.voff0, (V_) + (size_t)(t) * 64 * DH * 2, RFL(c.dst0 + (sl))); glds16s(c.voff1, (V_) + (size_t)(t) * 64 * DH * 2, RFL(c.dst0 + (sl) + 1024)); } while (0)
#define DMA_X(X_, j, slA, slB) do { const unsigned d_ = RFL(c.xdst + ((wid < 4) ? (slA) : (slB))); \
    glds16s(c.xsrc[0], (X_) + 128 * (j), d_); glds16s(c.xsrc[1], (X_) + 128 * (j), d_ + 1024); \
    glds16s(c.xsrc[2], (X_) + 128 * (j), d_ + 2048); glds16s(c.xsrc[3], (X_) + 128 * (j), d_ + 3072); } while (0)
#define DMA_WX(W_, X_, j, it_) do { DMA_W(W_, j, SLOT(it_, 3 * (j))); DMA_X(X_, j, SLOT(it_, 3 * (j) + 1), SLOT(it_, 3 * (j) + 2)); } while (0)
#define WAITBAR(N) asm volatile("s_waitcnt vmcnt(" #N ")\n\ts_barrier" ::: "memory")
#define REP16(M, ...) M(__VA_ARGS__, 0); M(__VA_ARGS__, 1); M(__VA_ARGS__, 2); M(__VA_ARGS__, 3); M(__VA_ARGS__, 4); M(__VA_ARGS__, 5); M(__VA_ARGS__, 6); M(__VA_ARGS__, 7); \
                      M(__VA_ARGS__, 8); M(__VA_ARGS__, 9); M(__VA_ARGS__, 10); M(__VA_ARGS__, 11); M(__VA_ARGS__, 12); M(__VA_ARGS__, 13); M(__VA_ARGS__, 14); M(__VA_ARGS__, 15)

template <int IT>
DI void attn_item(const char* lds, const LaneC& c, const int wid, const int hi,
                  const char* x, const char* Kp, const char* Vp, const char* wq_cur, const char* wq_next, const char* x_next,
                  uint16_t* __restrict__ Odst, const bool qvalid, unsigned long long* stamp_buf) {
  constexpr bool has_next = (IT == 0);
  STAMP(8 * IT + 0);
  s16x4 vlo[3], vhi[3];
#define VRD(i) do { vlo[(i) % 3] = vtr(vp_ + ((i) >> 2) * 4096 + ((i) & 3) * 512); vhi[(i) % 3] = vtr(vp_ + ((i) >> 2) * 4096 + 2048 + ((i) & 3) * 512); } while (0)
  bf16x8 qr[8];
  {
    f32x16 qa[4];
#pragma unroll
    for (int i = 0; i < 4; ++i) qa[i] = f32x16{};
    bf16x8 xf[4];
#define QGAP(i) do { if ((i) + 2 < 16) VRD((i) + 2); SBAR(); \
      qa[(i) & 3] = MFMA32(comb(vlo[(i) % 3], vhi[(i) % 3]), xf[(i) >> 2], qa[(i) & 3]); SBAR(); } while (0)
#define QGAP1(dummy, i) QGAP(i)
#define QSTEP(j, NW, ISSUE) do { \
      WAITBAR(NW); \
      const lds_cptr vp_ = (lds_cptr)lds + SLOT(IT, 3 * (j)) + c.vrd; \
      const lds_cptr xp_ = (lds_cptr)lds + ((wid < 4) ? SLOT(IT, 3 * (j) + 1) : SLOT(IT, 3 * (j) + 2)); \
      VRD(0); xf[0] = *(const __attribute__((address_space(3))) bf16x8*)(xp_ + c.xaddr[0]); VRD(1); \
      _Pragma("unroll") for (int ks = 1; ks < 4; ++ks) xf[ks] = *(const __attribute__((address_space(3))) bf16x8*)(xp_ + c.xaddr[ks]); \
      ISSUE; SBAR(); \
      REP16(QGAP1, 0); \
    } while (0)
    if constexpr (IT == 0) { QSTEP(0, 14, {}); } else { QSTEP(0, 8, DMA_W(wq_cur, 3, SLOT(IT, 9))); }
    QSTEP(1, 8, { DMA_X(x, 3, SLOT(IT, 10), SLOT(IT, 11)); DMA_W(wq_cur, 4, SLOT(IT, 12)); });
    QSTEP(2, 8, { DMA_X(x, 4, SLOT(IT, 13), SLOT(IT, 14)); DMA_W(wq_cur, 5, SLOT(IT, 15)); });
    QSTEP(3, 8, { DMA_X(x, 5, SLOT(IT, 16), SLOT(IT, 17)); DMA_W(wq_cur, 6, SLOT(IT, 18)); });
    QSTEP(4, 8, { DMA_X(x, 6, SLOT(IT, 19), SLOT(IT, 20)); DMA_W(wq_cur, 7, SLOT(IT, 21)); });
    QSTEP(5, 8, { DMA_X(x, 7, SLOT(IT, 22), SLOT(IT, 23)); DMA_W(wq_cur, 8, SLOT(IT, 24)); });
    QSTEP(6, 8, { DMA_X(x, 8, SLOT(IT, 25), SLOT(IT, 26)); DMA_W(wq_cur, 9, SLOT(IT, 27)); });
    QSTEP(7, 8, { DMA_X(x, 9, SLOT(IT, 28), SLOT(IT, 29)); DMA_K(Kp, 0, SLOT(IT, 30)); });
    QSTEP(8, 8, { DMA_K(Kp, 1, SLOT(IT, 31)); DMA_V(Vp, 0, SLOT(IT, 32)); DMA_K(Kp, 2, SLOT(IT, 33)); });
    QSTEP(9, 8, { DMA_V(Vp, 1, SLOT(IT, 34)); DMA_K(Kp, 3, SLOT(IT, 35)); DMA_V(Vp, 2, SLOT(IT, 36)); });
#undef QSTEP
#undef QGAP
#undef QGAP1
#pragma unroll
    for (int t = 0; t < 4; ++t) {
#pragma unroll
      for (int e = 0; e < 2; ++e) {
        const int B0 = 8 * e;
        unsigned a0 = cvtpk(qa[t][B0 + 0] * QSCALE, qa[t][B0 + 1] * QSCALE), a1 = cvtpk(qa[t][B0 + 2] * QSCALE, qa[t][B0 + 3] * QSCALE);
        unsigned b0 = cvtpk(qa[t][B0 + 4] * QSCALE, qa[t][B0 + 5] * QSCALE), b1 = cvtpk(qa[t][B0 + 6] * QSCALE, qa[t][B0 + 7] * QSCALE);
        auto r0 = __builtin_amdgcn_permlane32_swap(a0, b0, false, false);
        auto r1 = __builtin_amdgcn_permlane32_swap(a1, b1, false, false);
        u32x4 w = {r0[0], r1[0], r0[1], r1[1]};
        qr[2 * t + e] = __builtin_bit_cast(bf16x8, w);
      }
    }
  }
  STAMP(8 * IT + 1);

  f32x16 o[4];
#pragma unroll
  for (int i = 0; i < 4; ++i) o[i] = f32x16{};
  float mhat = 0.f, l_reg = 0.f, fres = 1.f;
  bool resc = false;
  f32x16 pA0, pA1, pB0, pB1;
  bf16x8 kf[3];
  u32x4 pw[4];

#define KLD(i) (*(const __attribute__((address_space(3))) bf16x8*)(kp_ + c.kaddr[(i) >> 1] + ((i) & 1) * 8192))
#define PACKP(P0, P1, i) do { if ((i) < 8) pw[(i) >> 2][(i) & 3] = cvtpk(P0[2 * (i)], P0[2 * (i) + 1]); \
                              else pw[2 + (((i) - 8) >> 2)][((i) - 8) & 3] = cvtpk(P1[2 * ((i) - 8)], P1[2 * ((i) - 8) + 1]); } while (0)
#define QKM(C0, C1, i) do { if ((i) & 1) { C1 = MFMA32(kf[(i) % 3], qr[(i) >> 1], ((i) < 2) ? f32x16{} : C1); } \
                            else { C0 = MFMA32(kf[(i) % 3], qr[(i) >> 1], ((i) < 2) ? f32x16{} : C0); } } while (0)
#define GAPA(C0, C1, P0, P1, i) do { if ((i) + 2 < 16) kf[((i) + 2) % 3] = KLD((i) + 2); SBAR(); \
    QKM(C0, C1, i); s0 += P0[i]; s1 += P1[i]; PACKP(P0, P1, i); PIN(pw[(i) >> 2]); PIN(s0); PIN(s1); SBAR(); } while (0)
#define EXP1(v) __builtin_amdgcn_exp2f((v) - mh)
#define GAPB(C0, C1, i) do { if ((i) + 2 < 16) VRD((i) + 2); SBAR(); \
    o[(i) & 3] = MFMA32(comb(vlo[(i) % 3], vhi[(i) % 3]), __builtin_bit_cast(bf16x8, pw[(i) >> 2]), o[(i) & 3]); \
    if ((i) < 8) { C0[2 * (i)] = EXP1(C0[2 * (i)]); C0[2 * (i) + 1] = EXP1(C0[2 * (i) + 1]); PIN(C0); } \
    else { C1[2 * ((i) - 8)] = EXP1(C1[2 * ((i) - 8)]); C1[2 * ((i) - 8) + 1] = EXP1(C1[2 * ((i) - 8) + 1]); PIN(C1); } SBAR(); } while (0)

  WAITBAR(12);
  DMA_K(Kp, 4, SLOT(IT, 37)); DMA_V(Vp, 3, SLOT(IT, 38)); DMA_K(Kp, 5, SLOT(IT, 39));
  STAMP(8 * IT + 2);
  {
    const lds_cptr kp_ = (lds_cptr)lds + SLOT(IT, 30);
#pragma unroll
    for (int i = 0; i < 16; ++i) { kf[i % 3] = KLD(i); QKM(pA0, pA1, i); }
    const float rm = rowmax32(pA0, pA1);
    mhat = rm;
    const float mh = mhat;
#pragma unroll
    for (int r = 0; r < 16; ++r) { pA0[r] = EXP1(pA0[r]); pA1[r] = EXP1(pA1[r]); }
  }

#define STEP(C0, C1, P0, P1, t, ISSUE) do { \
    const lds_cptr kp_ = (lds_cptr)lds + SLOT(IT, 2 * (t) + 29); \
    const lds_cptr vp_ = (lds_cptr)lds + SLOT(IT, 2 * (t) + 30) + c.vrd; \
    kf[0] = KLD(0); kf[1] = KLD(1); \
    ISSUE; \
    float s0 = 0.f, s1 = 0.f; SBAR(); \
    REP16(GAPA, C0, C1, P0, P1); \
    VRD(0); VRD(1); \
    l_reg += s0 + s1; \
    { const float rm = rowmax32(C0, C1) - mhat; resc = false; \
      if (__builtin_expect(__any(rm > THR), 0)) { const float dl = __builtin_fmaxf(rm, 0.f); mhat += dl; \
          fres = __builtin_amdgcn_exp2f(-dl); l_reg *= fres; resc = true; } } \
    const float mh = mhat; SBAR(); \
    REP16(GAPB, C0, C1); \
    if (resc) { _Pragma("unroll") for (int d_ = 0; d_ < 4; ++d_) _Pragma("unroll") for (int r = 0; r < 16; ++r) o[d_][r] *= fres; } \
  } while (0)

  WAITBAR(14);
  STAMP(8 * IT + 3);
  STEP(pB0, pB1, pA0, pA1, 1, DMA_V(Vp, 4, SLOT(IT, 40))); WAITBAR(12);
  for (int t = 2; t < 12; t += 2) {
    STEP(pA0, pA1, pB0, pB1, t,     { DMA_K(Kp, t + 4, SLOT(IT, 2 * t + 37)); DMA_V(Vp, t + 3, SLOT(IT, 2 * t + 38)); }); WAITBAR(12);
    STEP(pB0, pB1, pA0, pA1, t + 1, { DMA_K(Kp, t + 5, SLOT(IT, 2 * t + 39)); DMA_V(Vp, t + 4, SLOT(IT, 2 * t + 40)); }); WAITBAR(12);
  }
  STAMP(8 * IT + 4);
  if constexpr (has_next) {
    STEP(pA0, pA1, pB0, pB1, 12, { DMA_V(Vp, 15, SLOT(IT, 61)); DMA_W(wq_next, 0, SLOT(1, 0)); }); WAITBAR(12);
    STEP(pB0, pB1, pA0, pA1, 13, { DMA_X(x_next, 0, SLOT(1, 1), SLOT(1, 2)); }); WAITBAR(12);
    STEP(pA0, pA1, pB0, pB1, 14, { DMA_W(wq_next, 1, SLOT(1, 3)); if (wid < 4) DMA_X(x_next, 1, SLOT(1, 4), SLOT(1, 4)); });
    if (wid < 4) WAITBAR(12); else WAITBAR(8);
    STEP(pB0, pB1, pA0, pA1, 15, { if (wid >= 4) DMA_X(x_next, 1, SLOT(1, 5), SLOT(1, 5)); DMA_W(wq_next, 2, SLOT(1, 6)); });
    if (wid < 4) WAITBAR(8); else WAITBAR(12);
    DMA_X(x_next, 2, SLOT(1, 7), SLOT(1, 8));
  } else {
    STEP(pA0, pA1, pB0, pB1, 12, { DMA_V(Vp, 15, SLOT(IT, 61)); }); WAITBAR(10);
    STEP(pB0, pB1, pA0, pA1, 13, {}); WAITBAR(6);
    STEP(pA0, pA1, pB0, pB1, 14, {}); WAITBAR(2);
    STEP(pB0, pB1, pA0, pA1, 15, {}); WAITBAR(0);
  }
  STAMP(8 * IT + 5);
  {
    float s0 = 0.f, s1 = 0.f;
#pragma unroll
    for (int i = 0; i < 16; ++i) { s0 += pB0[i]; s1 += pB1[i]; PACKP(pB0, pB1, i); }
    l_reg += s0 + s1;
    const lds_cptr vp_ = (lds_cptr)lds + SLOT(IT, 61) + c.vrd;
#pragma unroll
    for (int i = 0; i < 16; ++i) {
      VRD(i);
      o[i & 3] = MFMA32(comb(vlo[i % 3], vhi[i % 3]), __builtin_bit_cast(bf16x8, pw[i >> 2]), o[i & 3]);
    }
  }
  {
    auto rr = __builtin_amdgcn_permlane32_swap(__float_as_uint(l_reg), __float_as_uint(l_reg), false, false);
    l_reg = __uint_as_float(rr[0]) + __uint_as_float(rr[1]);
  }
  const float rinv = 1.0f / l_reg;
  if (qvalid) {
#pragma unroll
    for (int db = 0; db < 4; ++db) {
#pragma unroll
      for (int g = 0; g < 4; ++g) {
        u32x2 w;
        w[0] = cvtpk(o[db][4 * g + 0] * rinv, o[db][4 * g + 1] * rinv);
        w[1] = cvtpk(o[db][4 * g + 2] * rinv, o[db][4 * g + 3] * rinv);
        *(u32x2*)(Odst + 32 * db + 8 * g) = w;
      }
    }
  }
  STAMP(8 * IT + 6);
#undef KLD
#undef VRD
#undef PACKP
#undef QKM
#undef GAPA
#undef EXP1
#undef GAPB
#undef STEP
}

__global__ __launch_bounds__(512, 2) void attn_kernel(
    const uint16_t* __restrict__ xA, const uint16_t* __restrict__ kA, const uint16_t* __restrict__ vA, const uint16_t* __restrict__ wqA,
    const uint16_t* __restrict__ xB, const uint16_t* __restrict__ kB, const uint16_t* __restrict__ vB, const uint16_t* __restrict__ wqB,
    uint16_t* __restrict__ Ows) {
  __shared__ __attribute__((aligned(16))) char lds[LDS_BYTES];
  const int tid = threadIdx.x, lane = tid & 63, r32 = lane & 31, hi = lane >> 5;
  const int wid = __builtin_amdgcn_readfirstlane(tid >> 6);
  const int bh = blockIdx.x;
  const int h = bh & 31, b = bh >> 5;
  const unsigned lds0 = (unsigned)(uintptr_t)lds;
  unsigned long long* stamp_buf = (unsigned long long*)((char*)Ows + (64u << 20));

  const int row8 = (lane & 31) >> 2, c4 = lane & 3, sub = lane >> 5;
  LaneC c;
  c.vrd = ((lane & 3) << 3) | (((lane >> 2) & 3) << 6) | (((lane >> 4) & 1) << 5) | ((lane >> 5) << 8);
  const int kk = 8 * wid + row8;
  const int kx = (kk & ~12) | ((kk & 4) << 1) | ((kk & 8) >> 1);
  c.woff = (unsigned)((kx * HD + 32 * sub + 8 * c4) * 2);
  {
    const int kr0 = 8 * wid + (lane >> 4), kr1 = kr0 + 4;
    c.koff0 = (unsigned)((kr0 * DH + (((lane & 15) ^ (kr0 & 15)) * 8)) * 2);
    c.koff1 = (unsigned)((kr1 * DH + (((lane & 15) ^ (kr1 & 15)) * 8)) * 2);
    c.voff0 = (unsigned)(((8 * wid + row8) * DH + 32 * sub + 8 * c4) * 2);
    c.voff1 = c.voff0 + 128;
  }
#pragma unroll
  for (int i = 0; i < 4; ++i) {
    const int row = 32 * wid + 8 * i + (lane >> 3);
    const int rc = row > SQ - 1 ? SQ - 1 : row;
    c.xsrc[i] = (unsigned)(rc * DMODEL * 2 + (((lane & 7) ^ ((row >> 1) & 7)) << 4));
    const int xr = 32 * wid + r32;
    c.xaddr[i] = (xr & 127) * 128 + (((2 * i + hi) ^ ((xr >> 1) & 7)) << 4);
  }
#pragma unroll
  for (int d0 = 0; d0 < 8; ++d0) c.kaddr[d0] = r32 * 256 + (((2 * d0 + hi) ^ (r32 & 15)) << 4);
  c.dst0 = lds0 + (2 * wid) * 1024;
  c.xdst = lds0 + (wid & 3) * 4096;

  const char* xa = (const char*)uni(xA + (size_t)b * SQ * DMODEL);
  const char* xbp = (const char*)uni(xB + (size_t)b * SQ * DMODEL);
  const char* ka = (const char*)uni(kA + (size_t)(b * NH + h) * SK * DH);
  const char* kb = (const char*)uni(kB + (size_t)(b * NH + h) * SK * DH);
  const char* va = (const char*)uni(vA + (size_t)(b * NH + h) * SK * DH);
  const char* vb = (const char*)uni(vB + (size_t)(b * NH + h) * SK * DH);
  const char* wa = (const char*)uni(wqA + h * DH);
  const char* wb = (const char*)uni(wqB + h * DH);
  const int q = 32 * wid + r32;
  uint16_t* OdA = Ows + ((size_t)b * SQ + q) * HD + h * DH + 4 * hi;
  uint16_t* OdB = OdA + (size_t)MROWS * HD;
  const bool qvalid = q < SQ;

  DMA_WX(wa, xa, 0, 0); DMA_WX(wa, xa, 1, 0); DMA_WX(wa, xa, 2, 0); DMA_W(wa, 3, SLOT(0, 9));
  attn_item<0>(lds, c, wid, hi, xa, ka, va, wa, wb, xbp, OdA, qvalid, stamp_buf);
  attn_item<1>(lds, c, wid, hi, xbp, kb, vb, wb, wb, xbp, OdB, qvalid, stamp_buf);
}

constexpr int G_BM = 64, G_BN = 160, G_BK = 64, G_NST = 4;
constexpr int G_AB = G_BM * G_BK * 2, G_BB = G_BK * G_BN * 2, G_STAGE = G_AB + G_BB;

__global__ __launch_bounds__(512, 2) void oproj_kernel(const uint16_t* __restrict__ Ows, const uint16_t* __restrict__ woA,
                                                      const uint16_t* __restrict__ woB, uint16_t* __restrict__ out) {
  __shared__ __attribute__((aligned(16))) char lds[6 * 64 * 80 * 4];
  const int tid = threadIdx.x, lane = tid & 63, r32 = lane & 31, hi = lane >> 5;
  const int wid = __builtin_amdgcn_readfirstlane(tid >> 6);
  const int L = (blockIdx.x & 7) * 30 + (blockIdx.x >> 3);
  const int mtg = L >> 2, nt = L & 3;
  const int blk = mtg >= 30 ? 1 : 0, mt = mtg - 30 * blk;
  const char* A = (const char*)uni(Ows + (size_t)mtg * G_BM * HD);
  const char* Bw = (const char*)uni((blk ? woB : woA) + nt * G_BN);
  const unsigned lds0 = (unsigned)(uintptr_t)lds;
  const int mh = wid & 1, kg = wid >> 1;
  unsigned soff[4], sinc[4];
  {
    const int row = 8 * wid + (lane >> 3);
    soff[0] = (unsigned)(row * HD * 2 + (((lane & 7) ^ ((row >> 1) & 7)) << 4));
    sinc[0] = G_BK * 2;
    const int row8 = (lane & 31) >> 2, c4 = lane & 3;
#pragma unroll
    for (int i = 1; i < 4; ++i) {
      const int st = 2 * (wid + 8 * (i - 1)) + (lane >> 5);
      const int kgr = st / 5, cs = st - 5 * kgr;
      const int kk = 8 * kgr + row8;
      const int kx = (kk & ~12) | ((kk & 4) << 1) | ((kk & 8) >> 1);
      soff[i] = (unsigned)((kx * DMODEL + 32 * cs + 8 * c4) * 2);
      sinc[i] = G_BK * DMODEL * 2;
    }
  }
  const unsigned d0 = lds0 + wid * 1024;
#define G_STG(kt) do { const unsigned sb_ = d0 + ((kt) & (G_NST - 1)) * G_STAGE; \
    glds16s(soff[0] + (kt) * sinc[0], A, RFL(sb_)); \
    glds16s(soff[1] + (kt) * sinc[1], Bw, RFL(sb_ + 8192)); glds16s(soff[2] + (kt) * sinc[2], Bw, RFL(sb_ + 16384)); \
    if (wid < 4) glds16s(soff[3] + (kt) * sinc[3], Bw, RFL(sb_ + 24576)); } while (0)
  const int vrd = ((lane & 3) << 3) | (((lane >> 2) & 3) << 6) | (((lane >> 4) & 1) << 5) | ((lane >> 5) << 8);
  const int arow = 32 * mh + r32;
  const int aaddr = arow * 128 + (((2 * kg + hi) ^ ((arow >> 1) & 7)) << 4);
  const int baddr = G_AB + vrd + kg * 5120;
  f32x16 acc[5];
#pragma unroll
  for (int i = 0; i < 5; ++i) acc[i] = f32x16{};
  constexpr int NKT = HD / G_BK;
  G_STG(0); G_STG(1); G_STG(2);
  for (int kt = 0; kt < NKT; ++kt) {
    if (kt + 3 <= NKT) { if (wid < 4) asm volatile("s_waitcnt vmcnt(8)" ::: "memory"); else asm volatile("s_waitcnt vmcnt(6)" ::: "memory"); }
    else if (kt + 2 == NKT) { if (wid < 4) asm volatile("s_waitcnt vmcnt(4)" ::: "memory"); else asm volatile("s_waitcnt vmcnt(3)" ::: "memory"); }
    else asm volatile("s_waitcnt vmcnt(0)" ::: "memory");
    asm volatile("s_barrier" ::: "memory");
    if (kt + 3 < NKT) G_STG(kt + 3);
    const lds_cptr sp = (lds_cptr)lds + (kt & (G_NST - 1)) * G_STAGE;
    const bf16x8 afr = *(const __attribute__((address_space(3))) bf16x8*)(sp + aaddr);
#pragma unroll
    for (int cs = 0; cs < 5; ++cs) {
      s16x4 lo = vtr(sp + baddr + cs * 512), hh = vtr(sp + baddr + 2560 + cs * 512);
      acc[cs] = MFMA32(comb(lo, hh), afr, acc[cs]);
    }
  }
#undef G_STG
  __syncthreads();
  float* red = (float*)lds;
  if (kg > 0) {
#pragma unroll
    for (int cs = 0; cs < 5; ++cs)
#pragma unroll
      for (int r = 0; r < 16; ++r) red[((wid - 2) * 80 + cs * 16 + r) * 64 + lane] = acc[cs][r];
  }
  __syncthreads();
  if (kg == 0) {
#pragma unroll
    for (int g = 0; g < 3; ++g)
#pragma unroll
      for (int cs = 0; cs < 5; ++cs)
#pragma unroll
        for (int r = 0; r < 16; ++r) acc[cs][r] += red[((2 * g + mh) * 80 + cs * 16 + r) * 64 + lane];
    const int m = mt * G_BM + arow;
    uint16_t* dst = out + (size_t)m * (2 * DMODEL) + blk * DMODEL + nt * G_BN + 4 * hi;
#pragma unroll
    for (int cs = 0; cs < 5; ++cs)
#pragma unroll
      for (int g = 0; g < 4; ++g) {
        u32x2 w;
        w[0] = cvtpk(acc[cs][4 * g + 0], acc[cs][4 * g + 1]);
        w[1] = cvtpk(acc[cs][4 * g + 2], acc[cs][4 * g + 3]);
        *(u32x2*)(dst + 32 * cs + 8 * g) = w;
      }
  }
}

extern "C" void kernel_launch(void* const* d_in, const int* in_sizes, int n_in, void* d_out, int out_size, void* d_ws, size_t ws_size,
                              hipStream_t stream) {
  const uint16_t* xA = (const uint16_t*)d_in[0];
  const uint16_t* kA = (const uint16_t*)d_in[1];
  const uint16_t* vA = (const uint16_t*)d_in[2];
  const uint16_t* wqA = (const uint16_t*)d_in[3];
  const uint16_t* woA = (const uint16_t*)d_in[4];
  const uint16_t* xB = (const uint16_t*)d_in[5];
  const uint16_t* kB = (const uint16_t*)d_in[6];
  const uint16_t* vB = (const uint16_t*)d_in[7];
  const uint16_t* wqB = (const uint16_t*)d_in[8];
  const uint16_t* woB = (const uint16_t*)d_in[9];
  uint16_t* Ows = (uint16_t*)d_ws;
  attn_kernel<<<dim3(256), dim3(512), 0, stream>>>(xA, kA, vA, wqA, xB, kB, vB, wqB, Ows);
  oproj_kernel<<<dim3(240), dim3(512), 0, stream>>>(Ows, woA, woB, (uint16_t*)d_out);
}
```

```cpp
#include <hip/hip_runtime.h>
#include <hip/hip_bf16.h>
#include <stdint.h>

typedef __attribute__((ext_vector_type(8))) short bf16x8;
typedef __attribute__((ext_vector_type(4))) short s16x4;
typedef __attribute__((ext_vector_type(16))) float f32x16;
typedef __attribute__((ext_vector_type(4))) unsigned u32x4;
typedef __attribute__((ext_vector_type(2))) unsigned u32x2;
typedef float f32x2_t __attribute__((ext_vector_type(2)));
typedef __bf16 bf16x2_t __attribute__((ext_vector_type(2)));
typedef __attribute__((address_space(3))) const char* lds_cptr;
typedef short v4i16_t __attribute__((ext_vector_type(4)));

#define DI __device__ __forceinline__
#define SBAR() __builtin_amdgcn_sched_barrier(0)
#define PIN(x) asm volatile("" : "+v"(x))
#define MFMA32(a, b, c) __builtin_amdgcn_mfma_f32_32x32x16_bf16((a), (b), (c), 0, 0, 0)
#define RFL(x) ((unsigned)__builtin_amdgcn_readfirstlane((int)(x)))

constexpr int NBATCH = 8, SQ = 240, DMODEL = 640, NH = 32, SK = 1024, DH = 128;
constexpr int MROWS = NBATCH * SQ;
constexpr int HD = NH * DH;
constexpr float QSCALE = 0.08838834764831845f * 1.4426950408889634f;

#define STAMP(i) do {} while (0)
#define TNOW(t_) do {} while (0)
DI int crow(int r, int hi) { return (r & 3) + 8 * (r >> 2) + 4 * hi; }
DI unsigned cvtpk(float lo, float hi) {
  f32x2_t v = {lo, hi};
  bf16x2_t b = __builtin_convertvector(v, bf16x2_t);
  return __builtin_bit_cast(unsigned, b);
}
DI void glds16(const void* g, unsigned lds_base) {
  unsigned sv;
  asm volatile("s_mov_b32 %0, m0\n\ts_mov_b32 m0, %2\n\ts_nop 0\n\tglobal_load_lds_dwordx4 %1, off\n\ts_mov_b32 m0, %0"
               : "=&s"(sv) : "v"(g), "s"(lds_base) : "memory");
}
DI s16x4 vtr(lds_cptr p) {
  return __builtin_bit_cast(s16x4, __builtin_amdgcn_ds_read_tr16_b64_v4i16((__attribute__((address_space(3))) v4i16_t*)p));
}
DI bf16x8 comb(s16x4 lo, s16x4 hi) { return (bf16x8){lo[0], lo[1], lo[2], lo[3], hi[0], hi[1], hi[2], hi[3]}; }
#define MX3(a, b, c) __builtin_fmaxf(__builtin_fmaxf((a), (b)), (c))
DI float rowmax32(const f32x16& p0, const f32x16& p1) {
  float a = MX3(p0[0], p0[1], p1[0]), b = MX3(p0[2], p0[3], p1[1]);
  a = MX3(a, p1[2], p1[3]);
#pragma unroll
  for (int r = 4; r < 16; r += 4) {
    a = MX3(a, p0[r], p0[r + 1]); b = MX3(b, p0[r + 2], p0[r + 3]);
    a = MX3(a, p1[r], p1[r + 1]); b = MX3(b, p1[r + 2], p1[r + 3]);
  }
  float m = __builtin_fmaxf(a, b);
  auto rr = __builtin_amdgcn_permlane32_swap(__float_as_uint(m), __float_as_uint(m), false, false);
  return __builtin_fmaxf(__uint_as_float(rr[0]), __uint_as_float(rr[1]));
}

constexpr int NSLOT = 10, SLOTB = 16384, LDS_BYTES = NSLOT * SLOTB;
constexpr float THR = 8.0f;

DI void glds16s(unsigned voff, const void* sbase, unsigned lds_base) {
  unsigned sv;
  asm volatile("s_mov_b32 %0, m0\n\ts_mov_b32 m0, %3\n\ts_nop 0\n\tglobal_load_lds_dwordx4 %1, %2\n\ts_mov_b32 m0, %0"
               : "=&s"(sv) : "v"(voff), "s"(sbase), "s"(lds_base) : "memory");
}
DI const void* uni(const void* p) {
  const unsigned long long v = (unsigned long long)p;
  const unsigned lo = RFL((unsigned)v), hi = RFL((unsigned)(v >> 32));
  return (const void*)(((unsigned long long)hi << 32) | lo);
}

struct LaneC {
  unsigned woff, koff0, koff1, voff0, voff1, dst0, xdst;
  unsigned xsrc[4];
  int kaddr[8];
  int xaddr[4];
  int vrd;
};

#define SLOT(it_, n_) ((unsigned)(((2 * (it_) + (n_)) % NSLOT) * SLOTB))
#define DMA_W(W_, j, sl) do { glds16s(c.woff, (W_) + (size_t)(j) * 64 * HD * 2, RFL(c.dst0 + (sl))); glds16s(c.woff + 128, (W_) + (size_t)(j) * 64 * HD * 2, RFL(c.dst0 + (sl) + 1024)); } while (0)
#define DMA_K(K_, t, sl) do { glds16s(c.koff0, (K_) + (size_t)(t) * 64 * DH * 2, RFL(c.dst0 + (sl))); glds16s(c.koff1, (K_) + (size_t)(t) * 64 * DH * 2, RFL(c.dst0 + (sl) + 1024)); } while (0)
#define DMA_V(V_, t, sl) do { glds16s(c.voff0, (V_) + (size_t)(t) * 64 * DH * 2, RFL(c.dst0 + (sl))); glds16s(c.voff1, (V_) + (size_t)(t) * 64 * DH * 2, RFL(c.dst0 + (sl) + 1024)); } while (0)
#define DMA_X(X_, j, slA, slB) do { const unsigned d_ = RFL(c.xdst + ((wid < 4) ? (slA) : (slB))); \
    glds16s(c.xsrc[0], (X_) + 128 * (j), d_); glds16s(c.xsrc[1], (X_) + 128 * (j), d_ + 1024); \
    glds16s(c.xsrc[2], (X_) + 128 * (j), d_ + 2048); glds16s(c.xsrc[3], (X_) + 128 * (j), d_ + 3072); } while (0)
#define DMA_WX(W_, X_, j, it_) do { DMA_W(W_, j, SLOT(it_, 3 * (j))); DMA_X(X_, j, SLOT(it_, 3 * (j) + 1), SLOT(it_, 3 * (j) + 2)); } while (0)
#define WAITBAR(N) asm volatile("s_waitcnt vmcnt(" #N ")\n\ts_barrier" ::: "memory")
#define REP16(M, ...) M(__VA_ARGS__, 0); M(__VA_ARGS__, 1); M(__VA_ARGS__, 2); M(__VA_ARGS__, 3); M(__VA_ARGS__, 4); M(__VA_ARGS__, 5); M(__VA_ARGS__, 6); M(__VA_ARGS__, 7); \
                      M(__VA_ARGS__, 8); M(__VA_ARGS__, 9); M(__VA_ARGS__, 10); M(__VA_ARGS__, 11); M(__VA_ARGS__, 12); M(__VA_ARGS__, 13); M(__VA_ARGS__, 14); M(__VA_ARGS__, 15)

template <int IT>
DI void attn_item(const char* lds, const LaneC& c, const int wid, const int hi,
                  const char* x, const char* Kp, const char* Vp, const char* wq_cur, const char* wq_next, const char* x_next,
                  uint16_t* __restrict__ Odst, const bool qvalid, unsigned long long* stamp_buf) {
  constexpr bool has_next = (IT == 0);
  STAMP(8 * IT + 0);
  s16x4 vlo[3], vhi[3];
#define VRD(i) do { vlo[(i) % 3] = vtr(vp_ + ((i) >> 2) * 4096 + ((i) & 3) * 512); vhi[(i) % 3] = vtr(vp_ + ((i) >> 2) * 4096 + 2048 + ((i) & 3) * 512); } while (0)
  bf16x8 qr[8];
  {
    f32x16 qa[4];
#pragma unroll
    for (int i = 0; i < 4; ++i) qa[i] = f32x16{};
    bf16x8 xf[4];
#define QGAP(i) do { if ((i) + 2 < 16) VRD((i) + 2); SBAR(); \
      qa[(i) & 3] = MFMA32(comb(vlo[(i) % 3], vhi[(i) % 3]), xf[(i) >> 2], qa[(i) & 3]); SBAR(); } while (0)
#define QGAP1(dummy, i) QGAP(i)
#define QSTEP(j, NW, ISSUE) do { \
      WAITBAR(NW); \
      const lds_cptr vp_ = (lds_cptr)lds + SLOT(IT, 3 * (j)) + c.vrd; \
      const lds_cptr xp_ = (lds_cptr)lds + ((wid < 4) ? SLOT(IT, 3 * (j) + 1) : SLOT(IT, 3 * (j) + 2)); \
      VRD(0); xf[0] = *(const __attribute__((address_space(3))) bf16x8*)(xp_ + c.xaddr[0]); VRD(1); \
      _Pragma("unroll") for (int ks = 1; ks < 4; ++ks) xf[ks] = *(const __attribute__((address_space(3))) bf16x8*)(xp_ + c.xaddr[ks]); \
      ISSUE; SBAR(); \
      REP16(QGAP1, 0); \
    } while (0)
    if constexpr (IT == 0) { QSTEP(0, 14, {}); } else { QSTEP(0, 8, DMA_W(wq_cur, 3, SLOT(IT, 9))); }
    QSTEP(1, 8, { DMA_X(x, 3, SLOT(IT, 10), SLOT(IT, 11)); DMA_W(wq_cur, 4, SLOT(IT, 12)); });
    QSTEP(2, 8, { DMA_X(x, 4, SLOT(IT, 13), SLOT(IT, 14)); DMA_W(wq_cur, 5, SLOT(IT, 15)); });
    QSTEP(3, 8, { DMA_X(x, 5, SLOT(IT, 16), SLOT(IT, 17)); DMA_W(wq_cur, 6, SLOT(IT, 18)); });
    QSTEP(4, 8, { DMA_X(x, 6, SLOT(IT, 19), SLOT(IT, 20)); DMA_W(wq_cur, 7, SLOT(IT, 21)); });
    QSTEP(5, 8, { DMA_X(x, 7, SLOT(IT, 22), SLOT(IT, 23)); DMA_W(wq_cur, 8, SLOT(IT, 24)); });
    QSTEP(6, 8, { DMA_X(x, 8, SLOT(IT, 25), SLOT(IT, 26)); DMA_W(wq_cur, 9, SLOT(IT, 27)); });
    QSTEP(7, 8, { DMA_X(x, 9, SLOT(IT, 28), SLOT(IT, 29)); DMA_K(Kp, 0, SLOT(IT, 30)); });
    QSTEP(8, 8, { DMA_K(Kp, 1, SLOT(IT, 31)); DMA_V(Vp, 0, SLOT(IT, 32)); DMA_K(Kp, 2, SLOT(IT, 33)); });
    QSTEP(9, 8, { DMA_V(Vp, 1, SLOT(IT, 34)); DMA_K(Kp, 3, SLOT(IT, 35)); DMA_V(Vp, 2, SLOT(IT, 36)); });
#undef QSTEP
#undef QGAP
#undef QGAP1
#pragma unroll
    for (int t = 0; t < 4; ++t) {
#pragma unroll
      for (int e = 0; e < 2; ++e) {
        const int B0 = 8 * e;
        unsigned a0 = cvtpk(qa[t][B0 + 0] * QSCALE, qa[t][B0 + 1] * QSCALE), a1 = cvtpk(qa[t][B0 + 2] * QSCALE, qa[t][B0 + 3] * QSCALE);
        unsigned b0 = cvtpk(qa[t][B0 + 4] * QSCALE, qa[t][B0 + 5] * QSCALE), b1 = cvtpk(qa[t][B0 + 6] * QSCALE, qa[t][B0 + 7] * QSCALE);
        auto r0 = __builtin_amdgcn_permlane32_swap(a0, b0, false, false);
        auto r1 = __builtin_amdgcn_permlane32_swap(a1, b1, false, false);
        u32x4 w = {r0[0], r1[0], r0[1], r1[1]};
        qr[2 * t + e] = __builtin_bit_cast(bf16x8, w);
      }
    }
  }
  STAMP(8 * IT + 1);

  f32x16 o[4];
#pragma unroll
  for (int i = 0; i < 4; ++i) o[i] = f32x16{};
  float mhat = 0.f, l_reg = 0.f, fres = 1.f;
  bool resc = false;
  f32x16 pA0, pA1, pB0, pB1;
  bf16x8 kf[3];
  u32x4 pw[4];

#define KLD(i) (*(const __attribute__((address_space(3))) bf16x8*)(kp_ + c.kaddr[(i) >> 1] + ((i) & 1) * 8192))
#define PACKP(P0, P1, i) do { if ((i) < 8) pw[(i) >> 2][(i) & 3] = cvtpk(P0[2 * (i)], P0[2 * (i) + 1]); \
                              else pw[2 + (((i) - 8) >> 2)][((i) - 8) & 3] = cvtpk(P1[2 * ((i) - 8)], P1[2 * ((i) - 8) + 1]); } while (0)
#define QKM(C0, C1, i) do { if ((i) & 1) { C1 = MFMA32(kf[(i) % 3], qr[(i) >> 1], ((i) < 2) ? f32x16{} : C1); } \
                            else { C0 = MFMA32(kf[(i) % 3], qr[(i) >> 1], ((i) < 2) ? f32x16{} : C0); } } while (0)
#define GAPA(C0, C1, P0, P1, i) do { if ((i) + 2 < 16) kf[((i) + 2) % 3] = KLD((i) + 2); SBAR(); \
    QKM(C0, C1, i); s0 += P0[i]; s1 += P1[i]; PACKP(P0, P1, i); PIN(pw[(i) >> 2]); PIN(s0); PIN(s1); SBAR(); } while (0)
#define EXP1(v) __builtin_amdgcn_exp2f((v) - mh)
#define GAPB(C0, C1, i) do { if ((i) + 2 < 16) VRD((i) + 2); SBAR(); \
    o[(i) & 3] = MFMA32(comb(vlo[(i) % 3], vhi[(i) % 3]), __builtin_bit_cast(bf16x8, pw[(i) >> 2]), o[(i) & 3]); \
    if ((i) < 8) { C0[2 * (i)] = EXP1(C0[2 * (i)]); C0[2 * (i) + 1] = EXP1(C0[2 * (i) + 1]); PIN(C0); } \
    else { C1[2 * ((i) - 8)] = EXP1(C1[2 * ((i) - 8)]); C1[2 * ((i) - 8) + 1] = EXP1(C1[2 * ((i) - 8) + 1]); PIN(C1); } SBAR(); } while (0)

  WAITBAR(12);
  DMA_K(Kp, 4, SLOT(IT, 37)); DMA_V(Vp, 3, SLOT(IT, 38)); DMA_K(Kp, 5, SLOT(IT, 39));
  STAMP(8 * IT + 2);
  {
    const lds_cptr kp_ = (lds_cptr)lds + SLOT(IT, 30);
#pragma unroll
    for (int i = 0; i < 16; ++i) { kf[i % 3] = KLD(i); QKM(pA0, pA1, i); }
    const float rm = rowmax32(pA0, pA1);
    mhat = rm;
    const float mh = mhat;
#pragma unroll
    for (int r = 0; r < 16; ++r) { pA0[r] = EXP1(pA0[r]); pA1[r] = EXP1(pA1[r]); }
  }

#define STEP(C0, C1, P0, P1, t, ISSUE) do { \
    const lds_cptr kp_ = (lds_cptr)lds + SLOT(IT, 2 * (t) + 29); \
    const lds_cptr vp_ = (lds_cptr)lds + SLOT(IT, 2 * (t) + 30) + c.vrd; \
    kf[0] = KLD(0); kf[1] = KLD(1); \
    ISSUE; \
    float s0 = 0.f, s1 = 0.f; SBAR(); \
    REP16(GAPA, C0, C1, P0, P1); \
    VRD(0); VRD(1); \
    l_reg += s0 + s1; \
    { const float rm = rowmax32(C0, C1) - mhat; resc = false; \
      if (__builtin_expect(__any(rm > THR), 0)) { const float dl = __builtin_fmaxf(rm, 0.f); mhat += dl; \
          fres = __builtin_amdgcn_exp2f(-dl); l_reg *= fres; resc = true; } } \
    const float mh = mhat; SBAR(); \
    REP16(GAPB, C0, C1); \
    if (resc) { _Pragma("unroll") for (int d_ = 0; d_ < 4; ++d_) _Pragma("unroll") for (int r = 0; r < 16; ++r) o[d_][r] *= fres; } \
  } while (0)

  WAITBAR(14);
  STAMP(8 * IT + 3);
  STEP(pB0, pB1, pA0, pA1, 1, DMA_V(Vp, 4, SLOT(IT, 40))); WAITBAR(12);
  for (int t = 2; t < 12; t += 2) {
    STEP(pA0, pA1, pB0, pB1, t,     { DMA_K(Kp, t + 4, SLOT(IT, 2 * t + 37)); DMA_V(Vp, t + 3, SLOT(IT, 2 * t + 38)); }); WAITBAR(12);
    STEP(pB0, pB1, pA0, pA1, t + 1, { DMA_K(Kp, t + 5, SLOT(IT, 2 * t + 39)); DMA_V(Vp, t + 4, SLOT(IT, 2 * t + 40)); }); WAITBAR(12);
  }
  STAMP(8 * IT + 4);
  if constexpr (has_next) {
    STEP(pA0, pA1, pB0, pB1, 12, { DMA_V(Vp, 15, SLOT(IT, 61)); DMA_W(wq_next, 0, SLOT(1, 0)); }); WAITBAR(12);
    STEP(pB0, pB1, pA0, pA1, 13, { DMA_X(x_next, 0, SLOT(1, 1), SLOT(1, 2)); }); WAITBAR(12);
    STEP(pA0, pA1, pB0, pB1, 14, { DMA_W(wq_next, 1, SLOT(1, 3)); if (wid < 4) DMA_X(x_next, 1, SLOT(1, 4), SLOT(1, 4)); });
    if (wid < 4) WAITBAR(12); else WAITBAR(8);
    STEP(pB0, pB1, pA0, pA1, 15, { if (wid >= 4) DMA_X(x_next, 1, SLOT(1, 5), SLOT(1, 5)); DMA_W(wq_next, 2, SLOT(1, 6)); });
    if (wid < 4) WAITBAR(8); else WAITBAR(12);
    DMA_X(x_next, 2, SLOT(1, 7), SLOT(1, 8));
  } else {
    STEP(pA0, pA1, pB0, pB1, 12, { DMA_V(Vp, 15, SLOT(IT, 61)); }); WAITBAR(10);
    STEP(pB0, pB1, pA0, pA1, 13, {}); WAITBAR(6);
    STEP(pA0, pA1, pB0, pB1, 14, {}); WAITBAR(2);
    STEP(pB0, pB1, pA0, pA1, 15, {}); WAITBAR(0);
  }
  STAMP(8 * IT + 5);
  {
    float s0 = 0.f, s1 = 0.f;
#pragma unroll
    for (int i = 0; i < 16; ++i) { s0 += pB0[i]; s1 += pB1[i]; PACKP(pB0, pB1, i); }
    l_reg += s0 + s1;
    const lds_cptr vp_ = (lds_cptr)lds + SLOT(IT, 61) + c.vrd;
#pragma unroll
    for (int i = 0; i < 16; ++i) {
      VRD(i);
      o[i & 3] = MFMA32(comb(vlo[i % 3], vhi[i % 3]), __builtin_bit_cast(bf16x8, pw[i >> 2]), o[i & 3]);
    }
  }
  {
    auto rr = __builtin_amdgcn_permlane32_swap(__float_as_uint(l_reg), __float_as_uint(l_reg), false, false);
    l_reg = __uint_as_float(rr[0]) + __uint_as_float(rr[1]);
  }
  const float rinv = 1.0f / l_reg;
  if (qvalid) {
#pragma unroll
    for (int db = 0; db < 4; ++db) {
#pragma unroll
      for (int g = 0; g < 4; ++g) {
        u32x2 w;
        w[0] = cvtpk(o[db][4 * g + 0] * rinv, o[db][4 * g + 1] * rinv);
        w[1] = cvtpk(o[db][4 * g + 2] * rinv, o[db][4 * g + 3] * rinv);
        *(u32x2*)(Odst + (size_t)(db >> 1) * MROWS * 64 + 32 * (db & 1) + 8 * g) = w;
      }
    }
  }
  STAMP(8 * IT + 6);
#undef KLD
#undef VRD
#undef PACKP
#undef QKM
#undef GAPA
#undef EXP1
#undef GAPB
#undef STEP
}

__global__ __launch_bounds__(512, 2) void attn_kernel(
    const uint16_t* __restrict__ xA, const uint16_t* __restrict__ kA, const uint16_t* __restrict__ vA, const uint16_t* __restrict__ wqA,
    const uint16_t* __restrict__ xB, const uint16_t* __restrict__ kB, const uint16_t* __restrict__ vB, const uint16_t* __restrict__ wqB,
    uint16_t* __restrict__ Ows) {
  __shared__ __attribute__((aligned(16))) char lds[LDS_BYTES];
  const int tid = threadIdx.x, lane = tid & 63, r32 = lane & 31, hi = lane >> 5;
  const int wid = __builtin_amdgcn_readfirstlane(tid >> 6);
  const int bh = blockIdx.x;
  const int h = bh & 31, b = bh >> 5;
  const unsigned lds0 = (unsigned)(uintptr_t)lds;
  unsigned long long* stamp_buf = (unsigned long long*)((char*)Ows + (64u << 20));

  const int row8 = (lane & 31) >> 2, c4 = lane & 3, sub = lane >> 5;
  LaneC c;
  c.vrd = ((lane & 3) << 3) | (((lane >> 2) & 3) << 6) | (((lane >> 4) & 1) << 5) | ((lane >> 5) << 8);
  const int kk = 8 * wid + row8;
  const int kx = (kk & ~12) | ((kk & 4) << 1) | ((kk & 8) >> 1);
  c.woff = (unsigned)((kx * HD + 32 * sub + 8 * c4) * 2);
  {
    const int kr0 = 8 * wid + (lane >> 4), kr1 = kr0 + 4;
    c.koff0 = (unsigned)((kr0 * DH + (((lane & 15) ^ (kr0 & 15)) * 8)) * 2);
    c.koff1 = (unsigned)((kr1 * DH + (((lane & 15) ^ (kr1 & 15)) * 8)) * 2);
    c.voff0 = (unsigned)(((8 * wid + row8) * DH + 32 * sub + 8 * c4) * 2);
    c.voff1 = c.voff0 + 128;
  }
#pragma unroll
  for (int i = 0; i < 4; ++i) {
    const int row = 32 * wid + 8 * i + (lane >> 3);
    const int rc = row > SQ - 1 ? SQ - 1 : row;
    c.xsrc[i] = (unsigned)(rc * DMODEL * 2 + (((lane & 7) ^ ((row >> 1) & 7)) << 4));
    const int xr = 32 * wid + r32;
    c.xaddr[i] = (xr & 127) * 128 + (((2 * i + hi) ^ ((xr >> 1) & 7)) << 4);
  }
#pragma unroll
  for (int d0 = 0; d0 < 8; ++d0) c.kaddr[d0] = r32 * 256 + (((2 * d0 + hi) ^ (r32 & 15)) << 4);
  c.dst0 = lds0 + (2 * wid) * 1024;
  c.xdst = lds0 + (wid & 3) * 4096;

  const char* xa = (const char*)uni(xA + (size_t)b * SQ * DMODEL);
  const char* xbp = (const char*)uni(xB + (size_t)b * SQ * DMODEL);
  const char* ka = (const char*)uni(kA + (size_t)(b * NH + h) * SK * DH);
  const char* kb = (const char*)uni(kB + (size_t)(b * NH + h) * SK * DH);
  const char* va = (const char*)uni(vA + (size_t)(b * NH + h) * SK * DH);
  const char* vb = (const char*)uni(vB + (size_t)(b * NH + h) * SK * DH);
  const char* wa = (const char*)uni(wqA + h * DH);
  const char* wb = (const char*)uni(wqB + h * DH);
  const int q = 32 * wid + r32;
  uint16_t* OdA = Ows + ((size_t)(2 * h) * MROWS + b * SQ + q) * 64 + 4 * hi;
  uint16_t* OdB = OdA + (size_t)MROWS * HD;
  const bool qvalid = q < SQ;

  DMA_WX(wa, xa, 0, 0); DMA_WX(wa, xa, 1, 0); DMA_WX(wa, xa, 2, 0); DMA_W(wa, 3, SLOT(0, 9));
  attn_item<0>(lds, c, wid, hi, xa, ka, va, wa, wb, xbp, OdA, qvalid, stamp_buf);
  attn_item<1>(lds, c, wid, hi, xbp, kb, vb, wb, wb, xbp, OdB, qvalid, stamp_buf);
}

constexpr int G_BM = 64, G_BN = 160, G_BK = 64, G_NST = 4;
constexpr int G_AB = G_BM * G_BK * 2, G_BB = G_BK * G_BN * 2, G_STAGE = G_AB + G_BB;

__global__ __launch_bounds__(512, 2) void oproj_kernel(const uint16_t* __restrict__ Ows, const uint16_t* __restrict__ woA,
                                                      const uint16_t* __restrict__ woB, uint16_t* __restrict__ out) {
  __shared__ __attribute__((aligned(16))) char lds[6 * 64 * 80 * 4];
  const int tid = threadIdx.x, lane = tid & 63, r32 = lane & 31, hi = lane >> 5;
  const int wid = __builtin_amdgcn_readfirstlane(tid >> 6);
  const int L = (blockIdx.x & 7) * 30 + (blockIdx.x >> 3);
  const int mtg = L >> 2, nt = L & 3;
  const int blk = mtg >= 30 ? 1 : 0, mt = mtg - 30 * blk;
  const char* A = (const char*)uni(Ows + (size_t)blk * MROWS * HD + (size_t)mt * G_BM * 64);
  const char* Bw = (const char*)uni((blk ? woB : woA) + nt * G_BN);
  const unsigned lds0 = (unsigned)(uintptr_t)lds;
  const int mh = wid & 1, kg = wid >> 1;
  unsigned soff[4], sinc[4];
  {
    const int row = 8 * wid + (lane >> 3);
    soff[0] = (unsigned)(row * 128 + (((lane & 7) ^ ((row >> 1) & 7)) << 4));
    sinc[0] = MROWS * 128;
    const int row8 = (lane & 31) >> 2, c4 = lane & 3;
#pragma unroll
    for (int i = 1; i < 4; ++i) {
      const int st = 2 * (wid + 8 * (i - 1)) + (lane >> 5);
      const int kgr = st / 5, cs = st - 5 * kgr;
      const int kk = 8 * kgr + row8;
      const int kx = (kk & ~12) | ((kk & 4) << 1) | ((kk & 8) >> 1);
      soff[i] = (unsigned)((kx * DMODEL + 32 * cs + 8 * c4) * 2);
      sinc[i] = G_BK * DMODEL * 2;
    }
  }
  const unsigned d0 = lds0 + wid * 1024;
#define G_STG(kt) do { const unsigned sb_ = d0 + ((kt) & (G_NST - 1)) * G_STAGE; \
    glds16s(soff[0] + (kt) * sinc[0], A, RFL(sb_)); \
    glds16s(soff[1] + (kt) * sinc[1], Bw, RFL(sb_ + 8192)); glds16s(soff[2] + (kt) * sinc[2], Bw, RFL(sb_ + 16384)); \
    if (wid < 4) glds16s(soff[3] + (kt) * sinc[3], Bw, RFL(sb_ + 24576)); } while (0)
  const int vrd = ((lane & 3) << 3) | (((lane >> 2) & 3) << 6) | (((lane >> 4) & 1) << 5) | ((lane >> 5) << 8);
  const int arow = 32 * mh + r32;
  const int aaddr = arow * 128 + (((2 * kg + hi) ^ ((arow >> 1) & 7)) << 4);
  const int baddr = G_AB + vrd + kg * 5120;
  f32x16 acc[5];
#pragma unroll
  for (int i = 0; i < 5; ++i) acc[i] = f32x16{};
  constexpr int NKT = HD / G_BK;
  G_STG(0); G_STG(1); G_STG(2);
  for (int kt = 0; kt < NKT; ++kt) {
    if (kt + 3 <= NKT) { if (wid < 4) asm volatile("s_waitcnt vmcnt(8)" ::: "memory"); else asm volatile("s_waitcnt vmcnt(6)" ::: "memory"); }
    else if (kt + 2 == NKT) { if (wid < 4) asm volatile("s_waitcnt vmcnt(4)" ::: "memory"); else asm volatile("s_waitcnt vmcnt(3)" ::: "memory"); }
    else asm volatile("s_waitcnt vmcnt(0)" ::: "memory");
    asm volatile("s_barrier" ::: "memory");
    if (kt + 3 < NKT) G_STG(kt + 3);
    const lds_cptr sp = (lds_cptr)lds + (kt & (G_NST - 1)) * G_STAGE;
    const bf16x8 afr = *(const __attribute__((address_space(3))) bf16x8*)(sp + aaddr);
#pragma unroll
    for (int cs = 0; cs < 5; ++cs) {
      s16x4 lo = vtr(sp + baddr + cs * 512), hh = vtr(sp + baddr + 2560 + cs * 512);
      acc[cs] = MFMA32(comb(lo, hh), afr, acc[cs]);
    }
  }
#undef G_STG
  __syncthreads();
  float* red = (float*)lds;
  if (kg > 0) {
#pragma unroll
    for (int cs = 0; cs < 5; ++cs)
#pragma unroll
      for (int r = 0; r < 16; ++r) red[((wid - 2) * 80 + cs * 16 + r) * 64 + lane] = acc[cs][r];
  }
  __syncthreads();
  if (kg == 0) {
#pragma unroll
    for (int g = 0; g < 3; ++g)
#pragma unroll
      for (int cs = 0; cs < 5; ++cs)
#pragma unroll
        for (int r = 0; r < 16; ++r) acc[cs][r] += red[((2 * g + mh) * 80 + cs * 16 + r) * 64 + lane];
    const int m = mt * G_BM + arow;
    uint16_t* dst = out + (size_t)m * (2 * DMODEL) + blk * DMODEL + nt * G_BN + 4 * hi;
#pragma unroll
    for (int cs = 0; cs < 5; ++cs)
#pragma unroll
      for (int g = 0; g < 4; ++g) {
        u32x2 w;
        w[0] = cvtpk(acc[cs][4 * g + 0], acc[cs][4 * g + 1]);
        w[1] = cvtpk(acc[cs][4 * g + 2], acc[cs][4 * g + 3]);
        *(u32x2*)(dst + 32 * cs + 8 * g) = w;
      }
  }
}

extern "C" void kernel_launch(void* const* d_in, const int* in_sizes, int n_in, void* d_out, int out_size, void* d_ws, size_t ws_size,
                              hipStream_t stream) {
  const uint16_t* xA = (const uint16_t*)d_in[0];
  const uint16_t* kA = (const uint16_t*)d_in[1];
  const uint16_t* vA = (const uint16_t*)d_in[2];
  const uint16_t* wqA = (const uint16_t*)d_in[3];
  const uint16_t* woA = (const uint16_t*)d_in[4];
  const uint16_t* xB = (const uint16_t*)d_in[5];
  const uint16_t* kB = (const uint16_t*)d_in[6];
  const uint16_t* vB = (const uint16_t*)d_in[7];
  const uint16_t* wqB = (const uint16_t*)d_in[8];
  const uint16_t* woB = (const uint16_t*)d_in[9];
  uint16_t* Ows = (uint16_t*)d_ws;
  attn_kernel<<<dim3(256), dim3(512), 0, stream>>>(xA, kA, vA, wqA, xB, kB, vB, wqB, Ows);
  oproj_kernel<<<dim3(240), dim3(512), 0, stream>>>(Ows, woA, woB, (uint16_t*)d_out);
}
```
